```python
import torch

def _softmax(x, axis=-1):
    return torch.softmax(x, dim=axis)

def _gelu(x):
    return torch.nn.functional.gelu(x, approximate='tanh')

def _silu(x):
    return torch.nn.functional.silu(x)

def _sigmoid(x):
    return torch.sigmoid(x)

def _layernorm(x, axis=-1, eps=1e-05):
    m = x.mean(dim=axis, keepdim=True)
    v = x.var(dim=axis, keepdim=True, unbiased=False)
    return (x - m) / torch.sqrt(v + eps)

def _scatter_add(tgt, idx, src, axis=0):
    return tgt.clone().index_add_(axis, idx, src)

def _segment_sum(src, seg, n):
    out = torch.zeros((n,) + src.shape[1:], dtype=src.dtype)
    return out.index_add_(0, seg, src)

def _pad(x, pw):
    flat = tuple((v for pair in reversed(pw) for v in pair))
    return torch.nn.functional.pad(x, flat)
kernel_name = 'standalone__swin_window_attn__vision'

def setup_inputs():

    _w = lambda a: (lambda t: t.to(torch.bfloat16) if t.is_floating_point() else t)(torch.as_tensor(a))
    H = W = 384 // 8 * 8
    C, win = (192, 8)
    return {'x': _w((lambda *s: torch.randn(*s, dtype=torch.bfloat16) * 0.2)(1, H, W, C)), 'wqkv': _w((lambda *s: torch.randn(*s, dtype=torch.bfloat16) * 0.2)(C, 3 * C)), 'wo': _w((lambda *s: torch.randn(*s, dtype=torch.bfloat16) * 0.05)(C, C))}

def reference(x, wqkv, wo):
    B, H, W, C = x.shape
    win = 8
    nh, nw = (H // win, W // win)
    xw = (lambda x, *d: x.permute(*d))(x.reshape(B, nh, win, nw, win, C), 0, 1, 3, 2, 4, 5)
    xw = xw.reshape(B * nh * nw, win * win, C)
    qkv = xw @ wqkv
    q, k, v = (qkv[..., :C].clone(), qkv[..., C:2 * C].clone(), qkv[..., 2 * C:].clone())
    scale = C ** (-0.5)
    p = _softmax(torch.einsum('bqc,bkc->bqk', q, k) * scale, axis=-1)
    o = torch.einsum('bqk,bkc->bqc', p, v) @ wo
    o = (lambda x, *d: x.permute(*d))(o.reshape(B, nh, nw, win, win, C), 0, 1, 3, 2, 4, 5)
    return o.reshape(B, H, W, C)
```

```cpp
#include <hip/hip_runtime.h>
#include <hip/hip_bf16.h>

typedef float    f32x16 __attribute__((ext_vector_type(16)));
typedef float    f32x2  __attribute__((ext_vector_type(2)));
typedef __bf16   bf16x8 __attribute__((ext_vector_type(8)));
typedef __bf16   bf16x2 __attribute__((ext_vector_type(2)));
typedef unsigned u32x4  __attribute__((ext_vector_type(4)));
typedef unsigned u32x3  __attribute__((ext_vector_type(3)));
#define DI __device__ __forceinline__
#define MFMA(a, b, c) __builtin_amdgcn_mfma_f32_32x32x16_bf16((a), (b), (c), 0, 0, 0)

constexpr int C = 192, HW = 384, NWIN = 48, NW = NWIN * NWIN;
constexpr unsigned ROWB = HW * C * 2;

DI unsigned cvtpk(float lo, float hi) {
  f32x2 v = {lo, hi}; bf16x2 b = __builtin_convertvector(v, bf16x2); return __builtin_bit_cast(unsigned, b);
}
DI bf16x8 pack8(const f32x16& x, int u) {
  u32x4 w;
  w[0] = cvtpk(x[8 * u + 0], x[8 * u + 1]); w[1] = cvtpk(x[8 * u + 2], x[8 * u + 3]);
  w[2] = cvtpk(x[8 * u + 4], x[8 * u + 5]); w[3] = cvtpk(x[8 * u + 6], x[8 * u + 7]);
  return __builtin_bit_cast(bf16x8, w);
}
DI int perm23(int r) { return (r & 0x13) | ((r & 4) << 1) | ((r & 8) >> 1); }

__global__ __launch_bounds__(64) void prep_kernel(const unsigned short* __restrict__ wqkv, const unsigned short* __restrict__ wo,
                                                  u32x4* __restrict__ frags) {
  const int b = blockIdx.x, which = b / 36, a = (b % 36) / 6, t = b % 6;
  const int lane = threadIdx.x, r = lane & 31, h = lane >> 5, pr = perm23(r);
  f32x16 acc; for (int i = 0; i < 16; ++i) acc[i] = 0.f;
  if (which == 0) {
    const unsigned short* pa = wqkv + (32 * a + pr) * 576 + 8 * h;
    const unsigned short* pb = wqkv + (32 * t + pr) * 576 + 192 + 8 * h;
    #pragma unroll
    for (int k = 0; k < 12; ++k) {
      bf16x8 fa = *(const bf16x8*)(pa + 16 * k), fb = *(const bf16x8*)(pb + 16 * k);
      acc = MFMA(fa, fb, acc);
    }
    const float sc = 0.07216878364870322f * 1.4426950408889634f;
    for (int i = 0; i < 16; ++i) acc[i] *= sc;
  } else {
    const unsigned short* pa = wqkv + (32 * a + pr) * 576 + 384 + 8 * h;
    const unsigned short* pb = wo + (8 * h) * 192 + 6 * r + t;
    #pragma unroll
    for (int k = 0; k < 12; ++k) {
      bf16x8 fa = *(const bf16x8*)(pa + 16 * k);
      u32x4 w;
      #pragma unroll
      for (int j = 0; j < 4; ++j) w[j] = (unsigned)pb[(16 * k + 2 * j) * 192] | ((unsigned)pb[(16 * k + 2 * j + 1) * 192] << 16);
      acc = MFMA(fa, __builtin_bit_cast(bf16x8, w), acc);
    }
  }
  #pragma unroll
  for (int u = 0; u < 2; ++u) frags[(which * 72 + t * 12 + 2 * a + u) * 64 + lane] = __builtin_bit_cast(u32x4, pack8(acc, u));
}

__global__ __launch_bounds__(256, 1) void swin_main(const char* __restrict__ x, const u32x4* __restrict__ frags, char* __restrict__ out) {
  extern __shared__ __attribute__((aligned(16))) char lds[];
  const int tid = threadIdx.x, lane = tid & 63, wid = __builtin_amdgcn_readfirstlane(tid >> 6), r = lane & 31, h = lane >> 5;
  #pragma unroll 4
  for (int i = 0; i < 36; ++i) {
    const int f = wid * 36 + i;
    *(u32x4*)(lds + f * 1024 + lane * 16) = frags[f * 64 + lane];
  }
  __syncthreads();
  const unsigned xoff = (r >> 3) * ROWB + (r & 7) * 384 + 16 * h;
  const unsigned ooff = h * 1536 + 12 * r;
  const char* wl = lds + lane * 16;
  const int gw = blockIdx.x * 4 + wid;
  for (int it = 0; it < 3; ++it) {
    int win;
    if (it < 2) win = gw + 1024 * it; else { if (wid != 0) break; win = 2048 + blockIdx.x; }
    const int wy = win / NWIN, wx = win % NWIN;
    const size_t wbase = (size_t)wy * 8 * ROWB + (size_t)wx * 8 * 384;
    const char* xb = x + wbase;
    bf16x8 X[2][12];
    #pragma unroll
    for (int tt = 0; tt < 2; ++tt)
      #pragma unroll
      for (int s = 0; s < 12; ++s) X[tt][s] = *(const bf16x8*)(xb + xoff + tt * 4 * ROWB + 32 * s);
    f32x16 S[2][2];
    #pragma unroll
    for (int a = 0; a < 2; ++a) for (int b = 0; b < 2; ++b) for (int i = 0; i < 16; ++i) S[a][b][i] = 0.f;
    #pragma unroll
    for (int t = 0; t < 6; ++t) {
      f32x16 T[2];
      for (int i = 0; i < 16; ++i) { T[0][i] = 0.f; T[1][i] = 0.f; }
      #pragma unroll
      for (int s = 0; s < 12; ++s) {
        const bf16x8 m = *(const bf16x8*)(wl + (t * 12 + s) * 1024);
        T[0] = MFMA(m, X[0][s], T[0]);
        T[1] = MFMA(m, X[1][s], T[1]);
      }
      #pragma unroll
      for (int u = 0; u < 2; ++u)
        #pragma unroll
        for (int qt = 0; qt < 2; ++qt) {
          const bf16x8 tp = pack8(T[qt], u);
          S[0][qt] = MFMA(X[0][2 * t + u], tp, S[0][qt]);
          S[1][qt] = MFMA(X[1][2 * t + u], tp, S[1][qt]);
        }
    }
    bf16x8 P[2][4];
    #pragma unroll
    for (int qt = 0; qt < 2; ++qt) {
      float mx = S[0][qt][0];
      #pragma unroll
      for (int i = 1; i < 16; ++i) mx = fmaxf(mx, S[0][qt][i]);
      #pragma unroll
      for (int i = 0; i < 16; ++i) mx = fmaxf(mx, S[1][qt][i]);
      mx = fmaxf(mx, __shfl_xor(mx, 32));
      float sum = 0.f;
      #pragma unroll
      for (int kt = 0; kt < 2; ++kt)
        #pragma unroll
        for (int i = 0; i < 16; ++i) { const float e = __builtin_amdgcn_exp2f(S[kt][qt][i] - mx); S[kt][qt][i] = e; sum += e; }
      sum += __shfl_xor(sum, 32);
      const float inv = __builtin_amdgcn_rcpf(sum);
      #pragma unroll
      for (int kt = 0; kt < 2; ++kt) {
        #pragma unroll
        for (int i = 0; i < 16; ++i) S[kt][qt][i] *= inv;
        P[qt][2 * kt] = pack8(S[kt][qt], 0); P[qt][2 * kt + 1] = pack8(S[kt][qt], 1);
      }
    }
    unsigned Opk[2][16][3];
    #pragma unroll
    for (int tp = 0; tp < 3; ++tp) {
      f32x16 O[2][2];
      #pragma unroll
      for (int tb = 0; tb < 2; ++tb) {
        const int t = 2 * tp + tb;
        f32x16 V[2];
        for (int i = 0; i < 16; ++i) { V[0][i] = 0.f; V[1][i] = 0.f; }
        #pragma unroll
        for (int s = 0; s < 12; ++s) {
          const bf16x8 n = *(const bf16x8*)(wl + (72 + t * 12 + s) * 1024);
          V[0] = MFMA(X[0][s], n, V[0]);
          V[1] = MFMA(X[1][s], n, V[1]);
        }
        for (int i = 0; i < 16; ++i) { O[tb][0][i] = 0.f; O[tb][1][i] = 0.f; }
        #pragma unroll
        for (int kt = 0; kt < 2; ++kt)
          #pragma unroll
          for (int u = 0; u < 2; ++u) {
            const bf16x8 vp = pack8(V[kt], u);
            O[tb][0] = MFMA(P[0][2 * kt + u], vp, O[tb][0]);
            O[tb][1] = MFMA(P[1][2 * kt + u], vp, O[tb][1]);
          }
      }
      #pragma unroll
      for (int qt = 0; qt < 2; ++qt)
        #pragma unroll
        for (int i = 0; i < 16; ++i) Opk[qt][i][tp] = cvtpk(O[0][qt][i], O[1][qt][i]);
    }
    char* ob = out + wbase;
    #pragma unroll
    for (int qt = 0; qt < 2; ++qt)
      #pragma unroll
      for (int i = 0; i < 16; ++i) {
        u32x3 w = {Opk[qt][i][0], Opk[qt][i][1], Opk[qt][i][2]};
        *(u32x3*)(ob + ooff + (4 * qt + (i >> 2)) * ROWB + (i & 3) * 384) = w;
      }
  }
}

extern "C" void kernel_launch(void* const* d_in, const int* in_sizes, int n_in,
                              void* d_out, int out_size, void* d_ws, size_t ws_size,
                              hipStream_t stream) {
  const char* x = (const char*)d_in[0];
  const unsigned short* wqkv = (const unsigned short*)d_in[1];
  const unsigned short* wo = (const unsigned short*)d_in[2];
  u32x4* frags = (u32x4*)d_ws;
  hipFuncSetAttribute((const void*)swin_main, hipFuncAttributeMaxDynamicSharedMemorySize, 147456);
  prep_kernel<<<72, 64, 0, stream>>>(wqkv, wo, frags);
  swin_main<<<256, 256, 147456, stream>>>(x, frags, (char*)d_out);
}
```

```cpp
#include <hip/hip_runtime.h>
#include <hip/hip_bf16.h>

typedef float    f32x16 __attribute__((ext_vector_type(16)));
typedef float    f32x4  __attribute__((ext_vector_type(4)));
typedef float    f32x2  __attribute__((ext_vector_type(2)));
typedef __bf16   bf16x8 __attribute__((ext_vector_type(8)));
typedef __bf16   bf16x2 __attribute__((ext_vector_type(2)));
typedef unsigned u32x4  __attribute__((ext_vector_type(4)));
typedef unsigned u32x2  __attribute__((ext_vector_type(2)));
#define DI __device__ __forceinline__
#define SB() __builtin_amdgcn_sched_barrier(0)
#define MEMFENCE() asm volatile("" ::: "memory")
#define MFMA32(a, b, c) __builtin_amdgcn_mfma_f32_32x32x16_bf16((a), (b), (c), 0, 0, 0)
#define MFMA16(a, b, c) __builtin_amdgcn_mfma_f32_16x16x32_bf16((a), (b), (c), 0, 0, 0)

constexpr int NWIN = 48;
constexpr unsigned ROWB = 384 * 192 * 2;
constexpr unsigned XBYTES = 384u * 384u * 384u;
constexpr int NPER = 9;
constexpr unsigned LDS_X = 0, LDS_T = 73728, LDS_V = 98304, LDS_F = 147456, LDS_TOTAL = 147456 + 128 + 4096, SLOT = 24576;
constexpr unsigned F_X = LDS_F, F_TV = LDS_F + 16, F_S = LDS_F + 32, F_FREE = LDS_F + 48, F_T = LDS_F + 64;
#define SAUX 16
#define APRIO 2

#define STAMP()
#define STARG
#define STPASS

DI void mfma16_acc(f32x4& d, const bf16x8& a, const bf16x8& b) { asm volatile("v_mfma_f32_16x16x32_bf16 %0, %1, %2, %0" : "+v"(d) : "v"(a), "v"(b)); }
DI void mfma16_zero(f32x4& d, const bf16x8& a, const bf16x8& b) { asm volatile("v_mfma_f32_16x16x32_bf16 %0, %1, %2, 0" : "=&v"(d) : "v"(a), "v"(b)); }
DI unsigned cvtpk(float lo, float hi) {
  f32x2 v = {lo, hi}; bf16x2 b = __builtin_convertvector(v, bf16x2); return __builtin_bit_cast(unsigned, b);
}
DI u32x4 pack8(const f32x16& x, int u) {
  u32x4 w;
  w[0] = cvtpk(x[8 * u + 0], x[8 * u + 1]); w[1] = cvtpk(x[8 * u + 2], x[8 * u + 3]);
  w[2] = cvtpk(x[8 * u + 4], x[8 * u + 5]); w[3] = cvtpk(x[8 * u + 6], x[8 * u + 7]);
  return w;
}
DI int perm23(int r) { return (r & 0x13) | ((r & 4) << 1) | ((r & 8) >> 1); }
DI int omap(int tau, int r16) { return tau < 8 ? 8 * r16 + tau : 128 + 4 * r16 + (tau - 8); }
DI void wg_barrier() { asm volatile("s_waitcnt lgkmcnt(0)\n\ts_barrier" ::: "memory"); }

typedef __attribute__((address_space(3))) volatile unsigned lds_vu32;
DI void flag_set(char* lds, unsigned addr, unsigned v) { MEMFENCE(); *(lds_vu32*)(lds + addr) = v; MEMFENCE(); }
DI unsigned flag_get(char* lds, unsigned addr) { return *(lds_vu32*)(lds + addr); }
#define SPINARG
#define SPIN(n)
DI void flag_wait(char* lds, unsigned addr, unsigned v, unsigned target SPINARG) {
  unsigned tmp;
  asm volatile("v_cmp_gt_u32 vcc, %3, %1\n\t"
               "s_cbranch_vccz 2f\n\t"
               "1:\n\t"
               "s_sleep 1\n\t"
               "ds_read_b32 %0, %2\n\t"
               "s_waitcnt lgkmcnt(0)\n\t"
               "v_cmp_gt_u32 vcc, %3, %0\n\t"
               "s_cbranch_vccnz 1b\n\t"
               "2:"
               : "=&v"(tmp) : "v"(v), "v"(addr + (unsigned)(size_t)(lds_vu32*)lds), "s"(target)
               : "vcc", "scc", "memory");
}

__global__ __launch_bounds__(64) void prep_kernel(const unsigned short* __restrict__ wqkv, const unsigned short* __restrict__ wo,
                                                  u32x4* __restrict__ frags) {
  const int b = blockIdx.x, which = b / 36, a = (b % 36) / 6, t = b % 6;
  const int lane = threadIdx.x, r = lane & 31, h = lane >> 5, pr = perm23(r);
  f32x16 acc; for (int i = 0; i < 16; ++i) acc[i] = 0.f;
  if (which == 0) {
    const unsigned short* pa = wqkv + (32 * a + pr) * 576 + 8 * h;
    const unsigned short* pb = wqkv + (32 * t + pr) * 576 + 192 + 8 * h;
    bf16x8 fa[12], fb[12];
    #pragma unroll
    for (int k = 0; k < 12; ++k) { fa[k] = *(const bf16x8*)(pa + 16 * k); fb[k] = *(const bf16x8*)(pb + 16 * k); }
    #pragma unroll
    for (int k = 0; k < 12; ++k) acc = MFMA32(fa[k], fb[k], acc);
    const float sc = 0.07216878364870322f * 1.4426950408889634f;
    for (int i = 0; i < 16; ++i) acc[i] *= sc;
  } else {
    const unsigned short* pa = wqkv + (32 * a + pr) * 576 + 384 + 8 * h;
    const unsigned short* pb = wo + (8 * h) * 192 + omap(2 * t + (r >> 4), r & 15);
    bf16x8 fa[12]; unsigned short e[12][8];
    #pragma unroll
    for (int k = 0; k < 12; ++k) {
      fa[k] = *(const bf16x8*)(pa + 16 * k);
      #pragma unroll
      for (int j = 0; j < 8; ++j) e[k][j] = pb[(16 * k + j) * 192];
    }
    #pragma unroll
    for (int k = 0; k < 12; ++k) {
      u32x4 w;
      #pragma unroll
      for (int j = 0; j < 4; ++j) w[j] = (unsigned)e[k][2 * j] | ((unsigned)e[k][2 * j + 1] << 16);
      acc = MFMA32(fa[k], __builtin_bit_cast(bf16x8, w), acc);
    }
  }
  #pragma unroll
  for (int u = 0; u < 2; ++u) frags[(which * 72 + t * 12 + 2 * a + u) * 64 + lane] = pack8(acc, u);
}

template <int ISV> DI u32x4 pack_half(const f32x16& a, int u) {
  if (ISV) { const int o = 4 * u; return u32x4{cvtpk(a[o], a[o + 1]), cvtpk(a[o + 2], a[o + 3]), cvtpk(a[o + 8], a[o + 9]), cvtpk(a[o + 10], a[o + 11])}; }
  else return pack8(a, u);
}
template <int ISV> DI void proj_wave(char* lds, const u32x4* __restrict__ frags, const int w2, const int lane STARG) {
  const int r = lane & 31, h = lane >> 5;
  __builtin_amdgcn_s_setprio(1);
  bf16x8 W[3][12];
  const u32x4* fp = frags + (ISV * 72 + w2 * 36) * 64 + lane;
  #pragma unroll
  for (int s = 0; s < 12; s += 2)
    #pragma unroll
    for (int t = 0; t < 3; ++t) { W[t][s] = __builtin_bit_cast(bf16x8, fp[(t * 12 + s) * 64]); W[t][s + 1] = __builtin_bit_cast(bf16x8, fp[(t * 12 + s + 1) * 64]); }
  SB();
  unsigned xa[4];
  #pragma unroll
  for (int s3 = 0; s3 < 4; ++s3) xa[s3] = LDS_X + r * 128 + (((2 * s3 + h) ^ ((r >> 1) & 7)) << 4);
  unsigned wa = ISV ? (LDS_V + (w2 * 6 + (r >> 4)) * 2048 + h * 256 + (r & 15) * 16)
                    : (LDS_T + ((r >> 4) * 6 + w2 * 3) * 1024 + h * 256 + (r & 15) * 16);
  unsigned wprev = wa + (ISV ? SLOT : 0);
  constexpr unsigned TS = ISV ? 4096 : 1024, HS = ISV ? 1024 : 12288;
  const unsigned fl = (lane & 3) * 4, wflag = F_TV + (ISV * 2 + w2) * 4;
  constexpr unsigned F_BUF = ISV ? F_FREE : F_T;
  STAMP();
  flag_wait(lds, F_X + fl, flag_get(lds, F_X + fl), 1 SPIN(0));
  STAMP();
  f32x16 acc[3];
  #pragma unroll
  for (int t = 0; t < 3; ++t) for (int e = 0; e < 16; ++e) acc[t][e] = 0.f;
  bf16x8 xc0 = *(const bf16x8*)(lds + xa[0]), xc1 = *(const bf16x8*)(lds + xa[1]), xd0 = *(const bf16x8*)(lds + xa[2]), xd1 = *(const bf16x8*)(lds + xa[3]);
  u32x4 pk0, pk1;
  pk0 = pack_half<ISV>(acc[1], 0);
  int xs = 0;
  for (int i = 0; i < NPER; ++i) {
    const unsigned dx = (xs == 2) ? -2 * SLOT : SLOT;
    xs = (xs == 2) ? 0 : xs + 1;
    const unsigned dv = ISV ? ((i & 1) ? -SLOT : SLOT) : 0;
    unsigned fv = 0;
    #pragma unroll
    for (int g = 0; g < 12; ++g) {
      const int tt = g / 6, sp = g % 6, s = 2 * sp;
      const bool first = sp == 0, last = sp == 5;
      const unsigned wpb = (tt == 0) ? wprev + HS : wa;
      const unsigned wcb = wa + tt * HS;
      bf16x8 xn0, xn1;
      const f32x16 zero = {0.f, 0.f, 0.f, 0.f, 0.f, 0.f, 0.f, 0.f, 0.f, 0.f, 0.f, 0.f, 0.f, 0.f, 0.f, 0.f};
      #define PM(t, x, ss, c) (ISV ? MFMA32(x, W[t][ss], c) : MFMA32(W[t][ss], x, c))
      acc[0] = PM(0, xc0, s, first ? zero : acc[0]);
      if (first) pk1 = pack_half<ISV>(acc[1], 1);
      if (g == 10) flag_wait(lds, F_X + fl, fv, i + 2 < NPER + 1 ? i + 2 : NPER SPIN(1));
      SB();
      acc[0] = PM(0, xc1, s + 1, acc[0]);
      if (first) { *(u32x4*)(lds + wpb + TS) = pk0; *(u32x4*)(lds + wpb + TS + 512) = pk1; pk0 = pack_half<ISV>(acc[2], 0); }
      {
        const int gn = (g + 2) % 12, ttn = gn / 6, sn = 2 * (gn % 6);
        const unsigned off = (sn >> 2) * 8192 + ttn * 4096;
        if (g >= 10) { xn0 = *(const bf16x8*)(lds + xa[sn & 3] + dx); xn1 = *(const bf16x8*)(lds + xa[(sn + 1) & 3] + dx); }
        else { xn0 = *(const bf16x8*)(lds + xa[sn & 3] + off); xn1 = *(const bf16x8*)(lds + xa[(sn + 1) & 3] + off); }
      }
      if (g == 5) fv = flag_get(lds, F_BUF + fl);
      SB();
      acc[1] = PM(1, xc0, s, first ? zero : acc[1]);
      if (first) pk1 = pack_half<ISV>(acc[2], 1);
      SB();
      acc[1] = PM(1, xc1, s + 1, acc[1]);
      if (first) { *(u32x4*)(lds + wpb + 2 * TS) = pk0; *(u32x4*)(lds + wpb + 2 * TS + 512) = pk1; }
      if (g == 0) flag_set(lds, wflag, i);
      if (last) pk0 = pack_half<ISV>(acc[0], 0);
      if (g == 9) fv = flag_get(lds, F_X + fl);
      SB();
      acc[2] = PM(2, xc0, s, first ? zero : acc[2]);
      if (last) pk1 = pack_half<ISV>(acc[0], 1);
      SB();
      acc[2] = PM(2, xc1, s + 1, acc[2]);
      if (g == 5) flag_wait(lds, F_BUF + fl, fv, ISV ? (i >= 1 ? i - 1 : 0) : i SPIN(2));
      if (last) { *(u32x4*)(lds + wcb) = pk0; *(u32x4*)(lds + wcb + 512) = pk1; pk0 = pack_half<ISV>(acc[1], 0); }
      SB();
      xc0 = xd0; xc1 = xd1; xd0 = xn0; xd1 = xn1;
    }
    #pragma unroll
    for (int s3 = 0; s3 < 4; ++s3) xa[s3] += dx;
    wprev = wa; wa += dv;
    STAMP();
  }
  {
    const unsigned wpb = wprev + HS;
    pk1 = pack_half<ISV>(acc[1], 1);
    *(u32x4*)(lds + wpb + TS) = pk0; *(u32x4*)(lds + wpb + TS + 512) = pk1;
    *(u32x4*)(lds + wpb + 2 * TS) = pack_half<ISV>(acc[2], 0); *(u32x4*)(lds + wpb + 2 * TS + 512) = pack_half<ISV>(acc[2], 1);
    flag_set(lds, wflag, NPER);
  }
}

DI float max3f(float a, float b, float c) { float r; asm("v_max3_f32 %0, %1, %2, %3" : "=v"(r) : "v"(a), "v"(b), "v"(c)); return r; }
DI float fmul_s(float a, float b) { float r; asm("v_mul_f32_e32 %0, %1, %2" : "=v"(r) : "v"(a), "v"(b)); return r; }
DI float max2f(float a, float b) { float r; asm("v_max_f32_e32 %0, %1, %2" : "=v"(r) : "v"(a), "v"(b)); return r; }
DI float xmax16(float v) { auto p = __builtin_amdgcn_permlane16_swap(__float_as_uint(v), __float_as_uint(v), false, false); return max2f(__uint_as_float(p[0]), __uint_as_float(p[1])); }
DI float xmax32(float v) { auto p = __builtin_amdgcn_permlane32_swap(__float_as_uint(v), __float_as_uint(v), false, false); return max2f(__uint_as_float(p[0]), __uint_as_float(p[1])); }
DI float xadd16(float v) { auto p = __builtin_amdgcn_permlane16_swap(__float_as_uint(v), __float_as_uint(v), false, false); return __uint_as_float(p[0]) + __uint_as_float(p[1]); }
DI float xadd32(float v) { auto p = __builtin_amdgcn_permlane32_swap(__float_as_uint(v), __float_as_uint(v), false, false); return __uint_as_float(p[0]) + __uint_as_float(p[1]); }

DI unsigned win_base(int win) { const int wy = win / NWIN, wx = win % NWIN; return (unsigned)wy * 8u * ROWB + (unsigned)wx * 3072u; }

template <int N> DI void wait_pf(u32x4 (&p)[6]) {
  asm volatile("s_waitcnt vmcnt(%6)" : "+v"(p[0]), "+v"(p[1]), "+v"(p[2]), "+v"(p[3]), "+v"(p[4]), "+v"(p[5]) : "i"(N) : "memory");
}

DI void attn_wave(char* lds, const char* __restrict__ x, char* __restrict__ out, const int a, const int lane STARG) {
  const int r16 = lane & 15, g = lane >> 4;
  auto rx = __builtin_amdgcn_make_buffer_rsrc((void*)x, (short)0, (int)XBYTES, 0x00020000);
  auto ro = __builtin_amdgcn_make_buffer_rsrc((void*)out, (short)0, (int)XBYTES, 0x00020000);
  const unsigned ld0 = 2 * a * ROWB + lane * 16, ld1 = ld0 + ROWB;
  const int b = blockIdx.x;
  u32x4 pf[3][6];
  auto issue = [&](u32x4 (&p)[6], int wi) {
    const unsigned wb = __builtin_amdgcn_readfirstlane(win_base(b + 256 * wi));
    asm volatile("s_cmp_ge_u32 %10, %11\n\t"
                 "s_cbranch_scc1 1f\n\t"
                 "s_nop 3\n\t"
                 "buffer_load_dwordx4 %0, %6, %8, %9 offen nt\n\t"
                 "buffer_load_dwordx4 %1, %6, %8, %9 offen offset:1024 nt\n\t"
                 "buffer_load_dwordx4 %2, %6, %8, %9 offen offset:2048 nt\n\t"
                 "buffer_load_dwordx4 %3, %7, %8, %9 offen nt\n\t"
                 "buffer_load_dwordx4 %4, %7, %8, %9 offen offset:1024 nt\n\t"
                 "buffer_load_dwordx4 %5, %7, %8, %9 offen offset:2048 nt\n\t"
                 "1:"
                 : "+v"(p[0]), "+v"(p[1]), "+v"(p[2]), "+v"(p[3]), "+v"(p[4]), "+v"(p[5])
                 : "v"(ld0), "v"(ld1), "s"(rx), "s"(wb), "s"(wi), "i"(NPER) : "memory", "scc");
  };
  #pragma unroll
  for (int q = 0; q < 3; ++q)
    #pragma unroll
    for (int m = 0; m < 6; ++m) asm volatile("" : "=v"(pf[q][m]));
  issue(pf[0], 0);
  SB();
  wg_barrier();
  unsigned xw[2][3];
  #pragma unroll
  for (int p = 0; p < 2; ++p)
    #pragma unroll
    for (int m = 0; m < 3; ++m) {
      const int n = 64 * m + lane, tx = n / 24, c = n % 24, tok = 8 * (2 * a + p) + tx;
      xw[p][m] = LDS_X + (c >> 3) * 8192 + tok * 128 + (((c & 7) ^ ((tok >> 1) & 7)) << 4);
    }
  unsigned xr[2];
  #pragma unroll
  for (int tp = 0; tp < 2; ++tp) xr[tp] = LDS_X + r16 * 128 + (((4 * tp + g) ^ ((r16 >> 1) & 7)) << 4);
  const unsigned ta = LDS_T + a * 6144 + lane * 16, va = LDS_V + lane * 16;
  const unsigned so4 = (2 * a + (g >> 1)) * ROWB + 4 * (g & 1) * 384 + 16 * r16, so2 = (2 * a + (g >> 1)) * ROWB + 4 * (g & 1) * 384 + 256 + 8 * r16;
  const unsigned fl = (lane & 3) * 4;
  auto stage = [&](u32x4 (&p)[6], unsigned lslot) {
    #pragma unroll
    for (int pp = 0; pp < 2; ++pp)
      #pragma unroll
      for (int m = 0; m < 3; ++m) *(u32x4*)(lds + xw[pp][m] + lslot) = p[3 * pp + m];
  };
  f32x4 O[12];
  u32x4 w4; u32x2 w2;
  auto epi = [&](int q, unsigned wb) {
    const int e = q >> 3, p = q & 7;
    if (p < 4) { w4[p] = cvtpk(O[2 * p][e], O[2 * p + 1][e]); asm volatile("" : "+v"(w4[p])); }
    else if (p < 6) { w2[p - 4] = cvtpk(O[2 * p][e], O[2 * p + 1][e]); asm volatile("" : "+v"(w2[p - 4])); }
    else if (p == 6) __builtin_amdgcn_raw_buffer_store_b128(w4, ro, so4 + e * 384, wb, SAUX);
    else __builtin_amdgcn_raw_buffer_store_b64(w2, ro, so2 + e * 384, wb, SAUX);
  };
  #pragma unroll
  for (int n = 0; n < 12; ++n) O[n] = f32x4{0.f, 0.f, 0.f, 0.f};
  STAMP();
  wait_pf<0>(pf[0]); stage(pf[0], 0);
  flag_set(lds, F_X + a * 4, 1);
  STAMP();
  issue(pf[1], 1); issue(pf[2], 2);
  wait_pf<6>(pf[1]); stage(pf[1], SLOT);
  flag_set(lds, F_X + a * 4, 2);
  issue(pf[0], 3);
  wait_pf<6>(pf[2]); stage(pf[2], 2 * SLOT);
  flag_set(lds, F_X + a * 4, 3);
  issue(pf[1], 4); issue(pf[2], 5);
  STAMP();
  unsigned wbprev = XBYTES;
  for (int i3 = 0; i3 < 3; ++i3) {
    #pragma unroll
    for (int k = 0; k < 3; ++k) {
      const int j = 3 * i3 + k;
      unsigned vsl = va + ((j & 1) ? SLOT : 0);
      asm volatile("" : "+v"(vsl));
      flag_wait(lds, F_TV + fl, flag_get(lds, F_TV + fl), j + 1 SPIN(1));
      STAMP();
      bf16x8 tf[6], xf[24];
      auto rdx = [&](int n) { const int t = n >> 2, kt = n & 3; xf[n] = *(const bf16x8*)(lds + xr[t & 1] + k * SLOT + (t >> 1) * 8192 + kt * 2048); };
      auto rdt = [&](int t) { tf[t] = *(const bf16x8*)(lds + ta + t * 1024); };
      rdt(0); rdt(1); rdt(2); rdt(3); rdt(4); rdt(5); SB(); rdx(0); SB(); rdx(1); SB(); rdx(2); SB(); rdx(3); SB(); rdx(4); SB(); rdx(5); SB(); rdx(6); SB(); rdx(7); SB(); rdx(8);
      SB();
      #pragma unroll
      for (int q = 0; q < 8; ++q) epi(q, wbprev);
      SB();
      f32x4 S[4];
      __builtin_amdgcn_s_setprio(2); SB();
      bf16x8 vf[24];
      auto rdv = [&](int n) { vf[n] = *(const bf16x8*)(lds + vsl + n * 1024); };
      #pragma unroll
      for (int n = 0; n < 24; ++n) {
        if (n < 4) mfma16_zero(S[n & 3], xf[n], tf[n >> 2]); else mfma16_acc(S[n & 3], xf[n], tf[n >> 2]);
        if (n == 0) flag_set(lds, F_T + a * 4, j + 1);
        if (n < 3) { rdx(2 * n + 9); rdx(2 * n + 10); } else if (n + 12 < 24) rdx(n + 12);
        epi(8 + n, wbprev);
        SB();
      }
      __builtin_amdgcn_s_setprio(0);
      flag_set(lds, F_S + a * 4, j + 1);
      #pragma unroll
      for (int n = 0; n < 8; ++n) rdv(n);
      SB();
      asm volatile("" : "+v"(S[0]), "+v"(S[1]), "+v"(S[2]), "+v"(S[3]));
      float m0 = max3f(S[0][0], S[0][1], S[0][2]), m1 = max3f(S[0][3], S[1][0], S[1][1]), m2 = max3f(S[1][2], S[1][3], S[2][0]), m3 = max3f(S[2][1], S[2][2], S[2][3]);
      m0 = max3f(m0, S[3][0], S[3][1]); m1 = max3f(m1, S[3][2], S[3][3]);
      float mx = max2f(max3f(m0, m1, m2), m3);
      mx = xmax32(xmax16(mx));
      asm volatile("" : "+v"(mx));
      SB();
      float sum0 = 0.f, sum1 = 0.f;
      #pragma unroll
      for (int kt = 0; kt < 4; ++kt) {
        #pragma unroll
        for (int e = 0; e < 4; ++e) { S[kt][e] = __builtin_amdgcn_exp2f(S[kt][e] - mx); if (e & 1) sum1 += S[kt][e]; else sum0 += S[kt][e]; }
        asm volatile("" : "+v"(S[kt]), "+v"(sum0), "+v"(sum1));
        rdv(8 + 4 * kt); rdv(9 + 4 * kt); rdv(10 + 4 * kt); rdv(11 + 4 * kt);
        SB();
      }
      unsigned fs = flag_get(lds, F_S + fl);
      float sum = xadd32(xadd16(sum0 + sum1));
      float inv = __builtin_amdgcn_rcpf(sum);
      asm volatile("s_nop 0" : "+v"(inv));
      bf16x8 P[2];
      #pragma unroll
      for (int m = 0; m < 2; ++m) {
        float q[8];
        #pragma unroll
        for (int e = 0; e < 8; ++e) q[e] = fmul_s(S[2 * m + (e >> 2)][e & 3], inv);
        u32x4 w = {cvtpk(q[0], q[1]), cvtpk(q[2], q[3]), cvtpk(q[4], q[5]), cvtpk(q[6], q[7])};
        P[m] = __builtin_bit_cast(bf16x8, w);
      }
      asm volatile("" : "+v"(P[0]), "+v"(P[1]));
      SB();
      STAMP();
      if (k == 0) asm volatile("s_cmp_lg_u32 %0, 0\n\ts_cbranch_scc1 1f\n\ts_waitcnt vmcnt(20)\n\t1:" :: "s"(i3) : "memory", "scc");
      wait_pf<24>(pf[k]);
      flag_wait(lds, F_S + fl, fs, j + 1 SPIN(2));
      SB();
      __builtin_amdgcn_s_setprio(2); SB();
      #pragma unroll
      for (int n = 0; n < 24; ++n) {
        const int tau = (n & 1) + 2 * (n >> 2), m = (n >> 1) & 1;
        if (m) mfma16_acc(O[tau], P[1], vf[2 * tau + 1]); else mfma16_zero(O[tau], P[0], vf[2 * tau]);
        if ((n & 3) == 1) *(u32x4*)(lds + xw[(n >> 2) / 3][(n >> 2) % 3] + k * SLOT) = pf[k][n >> 2];
        SB();
      }
      __builtin_amdgcn_s_setprio(0);
      flag_set(lds, F_X + a * 4, j + 4);
      flag_set(lds, F_FREE + a * 4, j + 1);
      issue(pf[k], j + 6);
      wbprev = win_base(b + 256 * j);
      asm volatile("s_nop 5" : "+v"(O[0]), "+v"(O[1]), "+v"(O[2]), "+v"(O[3]), "+v"(O[4]), "+v"(O[5]), "+v"(O[6]), "+v"(O[7]), "+v"(O[8]), "+v"(O[9]), "+v"(O[10]), "+v"(O[11]));
      SB();
      STAMP();
    }
  }
  #pragma unroll
  for (int q = 0; q < 32; ++q) epi(q, wbprev);
}

__global__ __launch_bounds__(512, 2) void swin_main(const char* __restrict__ x, const u32x4* __restrict__ frags, char* __restrict__ out) {
  extern __shared__ __attribute__((aligned(16))) char lds[];
  const int tid = threadIdx.x, lane = tid & 63, wid = __builtin_amdgcn_readfirstlane(tid >> 6);
  if (wid >= 4) attn_wave(lds, x, out, wid - 4, lane STPASS);
  else {
    if (wid == 0) *(lds_vu32*)(lds + LDS_F + (lane & 31) * 4) = 0;
    wg_barrier();
    if (wid >= 2) proj_wave<1>(lds, frags, wid - 2, lane STPASS);
    else proj_wave<0>(lds, frags, wid, lane STPASS);
  }
}

extern "C" void kernel_launch(void* const* d_in, const int* in_sizes, int n_in,
                              void* d_out, int out_size, void* d_ws, size_t ws_size,
                              hipStream_t stream) {
  const char* x = (const char*)d_in[0];
  const unsigned short* wqkv = (const unsigned short*)d_in[1];
  const unsigned short* wo = (const unsigned short*)d_in[2];
  u32x4* frags = (u32x4*)d_ws;
  (void)hipFuncSetAttribute((const void*)swin_main, hipFuncAttributeMaxDynamicSharedMemorySize, LDS_TOTAL);
  prep_kernel<<<72, 64, 0, stream>>>(wqkv, wo, frags);
  swin_main<<<256, 512, LDS_TOTAL, stream>>>(x, frags, (char*)d_out);
}
```

```cpp
#include <hip/hip_runtime.h>
#include <hip/hip_bf16.h>

typedef float    f32x4  __attribute__((ext_vector_type(4)));
typedef float    f32x2  __attribute__((ext_vector_type(2)));
typedef __bf16   bf16x8 __attribute__((ext_vector_type(8)));
typedef __bf16   bf16x2 __attribute__((ext_vector_type(2)));
typedef unsigned u32x4  __attribute__((ext_vector_type(4)));
typedef __attribute__((address_space(3))) volatile unsigned lds_vu32;
#define DI __device__ __forceinline__
#define SB() __builtin_amdgcn_sched_barrier(0)
#define MEMFENCE() asm volatile("" ::: "memory")
#define MFMA16(a, b, c) __builtin_amdgcn_mfma_f32_16x16x32_bf16((a), (b), (c), 0, 0, 0)

constexpr int NWIN = 48;
constexpr unsigned ROWB = 384 * 192 * 2;
constexpr unsigned XBYTES = 384u * 384u * 384u;
constexpr unsigned SLOT = 24576;
constexpr unsigned LDS_X = 0, LDS_T = 73728, LDS_V = 98304, LDS_F = 147456, LDS_TOTAL = 147456 + 256;
constexpr unsigned F_X = LDS_F, F_S = LDS_F + 16, F_V = LDS_F + 48, F_T = LDS_F + 64;

DI unsigned cvtpk(float lo, float hi) {
  f32x2 v = {lo, hi}; bf16x2 b = __builtin_convertvector(v, bf16x2); return __builtin_bit_cast(unsigned, b);
}
DI int omap(int tau, int r16) { return tau < 8 ? 8 * r16 + tau : 128 + 4 * r16 + (tau - 8); }
DI int pairmap(int b, int rho) { return 8 * (rho >> 2) + 4 * b + (rho & 3); }
DI unsigned win_base(int win) { const int wy = win / NWIN, wx = win % NWIN; return (unsigned)wy * 8u * ROWB + (unsigned)wx * 3072u; }

__global__ __launch_bounds__(64) void prep_kernel(const unsigned short* __restrict__ wqkv, const unsigned short* __restrict__ wo,
                                                  u32x4* __restrict__ frags) {
  const int f = blockIdx.x, which = f / 72, tile = (f % 72) / 6, s = f % 6;
  const int lane = threadIdx.x, r16 = lane & 15, g = lane >> 4;
  const unsigned short* pa0 = wqkv + (32 * s + pairmap(0, r16)) * 576 + (which ? 384 : 0) + 8 * g;
  const unsigned short* pa1 = pa0 + 4 * 576;
  bf16x8 fa0[6], fa1[6], fb[6];
  #pragma unroll
  for (int k = 0; k < 6; ++k) { fa0[k] = *(const bf16x8*)(pa0 + 32 * k); fa1[k] = *(const bf16x8*)(pa1 + 32 * k); }
  if (which == 0) {
    const unsigned short* pb = wqkv + (32 * (tile >> 1) + pairmap(tile & 1, r16)) * 576 + 192 + 8 * g;
    #pragma unroll
    for (int k = 0; k < 6; ++k) fb[k] = *(const bf16x8*)(pb + 32 * k);
  } else {
    const unsigned short* pb = wo + (8 * g) * 192 + omap(tile, r16);
    unsigned short e[6][8];
    #pragma unroll
    for (int k = 0; k < 6; ++k)
      #pragma unroll
      for (int j = 0; j < 8; ++j) e[k][j] = pb[(32 * k + j) * 192];
    #pragma unroll
    for (int k = 0; k < 6; ++k) {
      u32x4 w;
      #pragma unroll
      for (int j = 0; j < 4; ++j) w[j] = (unsigned)e[k][2 * j] | ((unsigned)e[k][2 * j + 1] << 16);
      fb[k] = __builtin_bit_cast(bf16x8, w);
    }
  }
  f32x4 d0 = {0.f, 0.f, 0.f, 0.f}, d1 = {0.f, 0.f, 0.f, 0.f};
  #pragma unroll
  for (int k = 0; k < 6; ++k) { d0 = MFMA16(fa0[k], fb[k], d0); d1 = MFMA16(fa1[k], fb[k], d1); }
  const float sc = which ? 1.f : 0.07216878364870322f * 1.4426950408889634f;
  u32x4 w = {cvtpk(d0[0] * sc, d0[1] * sc), cvtpk(d0[2] * sc, d0[3] * sc), cvtpk(d1[0] * sc, d1[1] * sc), cvtpk(d1[2] * sc, d1[3] * sc)};
  frags[f * 64 + lane] = w;
}

template <int ISV> DI void proj_wave(char* lds, const u32x4* __restrict__ frags, const int w2, const int lane) {
  const int r16 = lane & 15, g = lane >> 4;
  const unsigned lb = (unsigned)(size_t)(lds_vu32*)lds;
  auto rf = __builtin_amdgcn_make_buffer_rsrc((void*)frags, (short)0, 144 * 1024, 0x00020000);
  const unsigned wb = (ISV * 72 + w2 * 36) * 1024;
  const unsigned xa0 = lb + LDS_X + r16 * 128 + ((g ^ ((r16 >> 1) & 7)) << 4);
  const unsigned wa = lb + (ISV ? LDS_V + w2 * 12288 : LDS_T + w2 * 3072) + lane * 16;
  if constexpr (ISV) {
    asm volatile(
    "s_setprio 1\n\t"
    "v_mov_b32 v248, 1\n\t"
    "s_mov_b32 s61, 1\n\t"
    "s_add_u32 s65, %[wb], 0\n\t"
    "buffer_load_dwordx4 v[32:35], %[l16], %[rf], s65 offen\n\t"
    "buffer_load_dwordx4 v[36:39], %[l16], %[rf], s65 offen offset:1024\n\t"
    "buffer_load_dwordx4 v[40:43], %[l16], %[rf], s65 offen offset:2048\n\t"
    "buffer_load_dwordx4 v[44:47], %[l16], %[rf], s65 offen offset:3072\n\t"
    "s_add_u32 s65, %[wb], 4096\n\t"
    "buffer_load_dwordx4 v[48:51], %[l16], %[rf], s65 offen\n\t"
    "buffer_load_dwordx4 v[52:55], %[l16], %[rf], s65 offen offset:1024\n\t"
    "s_waitcnt lgkmcnt(0)\n\t"
    "s_barrier\n\t"
    "buffer_load_dwordx4 v[56:59], %[l16], %[rf], s65 offen offset:2048\n\t"
    "buffer_load_dwordx4 v[60:63], %[l16], %[rf], s65 offen offset:3072\n\t"
    "s_add_u32 s65, %[wb], 8192\n\t"
    "buffer_load_dwordx4 v[64:67], %[l16], %[rf], s65 offen\n\t"
    "buffer_load_dwordx4 v[68:71], %[l16], %[rf], s65 offen offset:1024\n\t"
    "buffer_load_dwordx4 v[72:75], %[l16], %[rf], s65 offen offset:2048\n\t"
    "buffer_load_dwordx4 v[76:79], %[l16], %[rf], s65 offen offset:3072\n\t"
    "s_add_u32 s65, %[wb], 12288\n\t"
    "buffer_load_dwordx4 v[80:83], %[l16], %[rf], s65 offen\n\t"
    "buffer_load_dwordx4 v[84:87], %[l16], %[rf], s65 offen offset:1024\n\t"
    "buffer_load_dwordx4 v[88:91], %[l16], %[rf], s65 offen offset:2048\n\t"
    "buffer_load_dwordx4 v[92:95], %[l16], %[rf], s65 offen offset:3072\n\t"
    "s_add_u32 s65, %[wb], 16384\n\t"
    "buffer_load_dwordx4 v[96:99], %[l16], %[rf], s65 offen\n\t"
    "buffer_load_dwordx4 v[100:103], %[l16], %[rf], s65 offen offset:1024\n\t"
    "buffer_load_dwordx4 v[104:107], %[l16], %[rf], s65 offen offset:2048\n\t"
    "buffer_load_dwordx4 v[108:111], %[l16], %[rf], s65 offen offset:3072\n\t"
    "s_add_u32 s65, %[wb], 20480\n\t"
    "buffer_load_dwordx4 v[112:115], %[l16], %[rf], s65 offen\n\t"
    "buffer_load_dwordx4 v[116:119], %[l16], %[rf], s65 offen offset:1024\n\t"
    "buffer_load_dwordx4 v[120:123], %[l16], %[rf], s65 offen offset:2048\n\t"
    "buffer_load_dwordx4 v[124:127], %[l16], %[rf], s65 offen offset:3072\n\t"
    "s_add_u32 s65, %[wb], 24576\n\t"
    "buffer_load_dwordx4 v[128:131], %[l16], %[rf], s65 offen\n\t"
    "buffer_load_dwordx4 v[132:135], %[l16], %[rf], s65 offen offset:1024\n\t"
    "buffer_load_dwordx4 v[136:139], %[l16], %[rf], s65 offen offset:2048\n\t"
    "buffer_load_dwordx4 v[140:143], %[l16], %[rf], s65 offen offset:3072\n\t"
    "s_add_u32 s65, %[wb], 28672\n\t"
    "buffer_load_dwordx4 v[144:147], %[l16], %[rf], s65 offen\n\t"
    "buffer_load_dwordx4 v[148:151], %[l16], %[rf], s65 offen offset:1024\n\t"
    "buffer_load_dwordx4 v[152:155], %[l16], %[rf], s65 offen offset:2048\n\t"
    "buffer_load_dwordx4 v[156:159], %[l16], %[rf], s65 offen offset:3072\n\t"
    "s_add_u32 s65, %[wb], 32768\n\t"
    "buffer_load_dwordx4 v[160:163], %[l16], %[rf], s65 offen\n\t"
    "buffer_load_dwordx4 v[164:167], %[l16], %[rf], s65 offen offset:1024\n\t"
    "buffer_load_dwordx4 v[168:171], %[l16], %[rf], s65 offen offset:2048\n\t"
    "buffer_load_dwordx4 v[172:175], %[l16], %[rf], s65 offen offset:3072\n\t"
    "v_mov_b32 v28, %[xa0]\n\t"
    "v_xor_b32 v29, 64, %[xa0]\n\t"
    "v_mov_b32 v30, %[wa]\n\t"
    "v_add_u32 v31, 0x6000, %[wa]\n\t"
    "v_mov_b32 v248, 1\n\t"
    "s_mov_b32 s61, 1\n\t"
    "s_mov_b32 s62, 2\n\t"
    "s_mov_b32 s63, 0\n\t"
    "s_mov_b32 s64, 24576\n\t"
    "ds_read_b32 v249, %[fl4] offset:0\n\t"
    "s_waitcnt lgkmcnt(0)\n\t"
    "v_cmp_gt_u32 vcc, s61, v249\n\t"
    "s_cbranch_vccnz .Lsv1%=\n\t"
    ".Lrv1%=:\n\t"
    "ds_read_b128 v[176:179], v28 offset:0\n\t"
    "ds_read_b128 v[180:183], v29 offset:0\n\t"
    "ds_read_b128 v[184:187], v28 offset:8192\n\t"
    "ds_read_b128 v[188:191], v29 offset:8192\n\t"
    "ds_read_b128 v[192:195], v28 offset:16384\n\t"
    "ds_read_b128 v[196:199], v29 offset:16384\n\t"
    "s_waitcnt lgkmcnt(0)\n\t"
    "s_waitcnt vmcnt(35)\n\t"
    "v_mfma_f32_16x16x32_bf16 v[12:15], v[176:179], v[32:35], 0\n\t"
    "s_waitcnt vmcnt(34)\n\t"
    "v_mfma_f32_16x16x32_bf16 v[12:15], v[180:183], v[36:39], v[12:15]\n\t"
    "s_waitcnt vmcnt(33)\n\t"
    "v_mfma_f32_16x16x32_bf16 v[12:15], v[184:187], v[40:43], v[12:15]\n\t"
    "s_waitcnt vmcnt(32)\n\t"
    "v_mfma_f32_16x16x32_bf16 v[12:15], v[188:191], v[44:47], v[12:15]\n\t"
    "s_waitcnt vmcnt(31)\n\t"
    "v_mfma_f32_16x16x32_bf16 v[12:15], v[192:195], v[48:51], v[12:15]\n\t"
    "ds_read_b32 v249, %[fl4] offset:32\n\t"
    "s_waitcnt vmcnt(30)\n\t"
    "v_mfma_f32_16x16x32_bf16 v[12:15], v[196:199], v[52:55], v[12:15]\n\t"
    "s_waitcnt vmcnt(29)\n\t"
    "v_mfma_f32_16x16x32_bf16 v[16:19], v[176:179], v[56:59], 0\n\t"
    "ds_read_b128 v[200:203], v28 offset:2048\n\t"
    "s_waitcnt vmcnt(28)\n\t"
    "v_mfma_f32_16x16x32_bf16 v[16:19], v[180:183], v[60:63], v[16:19]\n\t"
    "v_add_u32 v248, 1, v248\n\t"
    "s_waitcnt vmcnt(27)\n\t"
    "v_mfma_f32_16x16x32_bf16 v[16:19], v[184:187], v[64:67], v[16:19]\n\t"
    "s_waitcnt vmcnt(26)\n\t"
    "v_mfma_f32_16x16x32_bf16 v[16:19], v[188:191], v[68:71], v[16:19]\n\t"
    "s_waitcnt vmcnt(25)\n\t"
    "v_mfma_f32_16x16x32_bf16 v[16:19], v[192:195], v[72:75], v[16:19]\n\t"
    "s_waitcnt vmcnt(24)\n\t"
    "v_mfma_f32_16x16x32_bf16 v[16:19], v[196:199], v[76:79], v[16:19]\n\t"
    "ds_read_b128 v[204:207], v29 offset:2048\n\t"
    "s_waitcnt lgkmcnt(2)\n\t"
    "v_cmp_gt_u32 vcc, s61, v249\n\t"
    "s_cbranch_vccnz .Lsv2%=\n\t"
    ".Lrv2%=:\n\t"
    "s_waitcnt vmcnt(23)\n\t"
    "v_mfma_f32_16x16x32_bf16 v[20:23], v[176:179], v[80:83], 0\n\t"
    "ds_read_b128 v[208:211], v28 offset:10240\n\t"
    "s_waitcnt vmcnt(22)\n\t"
    "v_mfma_f32_16x16x32_bf16 v[20:23], v[180:183], v[84:87], v[20:23]\n\t"
    "v_cvt_pk_bf16_f32 v224, v12, v13\n\t"
    "s_waitcnt vmcnt(21)\n\t"
    "v_mfma_f32_16x16x32_bf16 v[20:23], v[184:187], v[88:91], v[20:23]\n\t"
    "v_cvt_pk_bf16_f32 v225, v14, v15\n\t"
    "s_waitcnt vmcnt(20)\n\t"
    "v_mfma_f32_16x16x32_bf16 v[20:23], v[188:191], v[92:95], v[20:23]\n\t"
    "ds_write_b64 v30, v[224:225] offset:0\n\t"
    "s_waitcnt vmcnt(19)\n\t"
    "v_mfma_f32_16x16x32_bf16 v[20:23], v[192:195], v[96:99], v[20:23]\n\t"
    "s_waitcnt vmcnt(18)\n\t"
    "v_mfma_f32_16x16x32_bf16 v[20:23], v[196:199], v[100:103], v[20:23]\n\t"
    "ds_read_b128 v[212:215], v29 offset:10240\n\t"
    "s_waitcnt vmcnt(17)\n\t"
    "v_mfma_f32_16x16x32_bf16 v[12:15], v[176:179], v[104:107], 0\n\t"
    "ds_read_b128 v[216:219], v28 offset:18432\n\t"
    "s_waitcnt vmcnt(16)\n\t"
    "v_mfma_f32_16x16x32_bf16 v[12:15], v[180:183], v[108:111], v[12:15]\n\t"
    "v_cvt_pk_bf16_f32 v226, v16, v17\n\t"
    "s_waitcnt vmcnt(15)\n\t"
    "v_mfma_f32_16x16x32_bf16 v[12:15], v[184:187], v[112:115], v[12:15]\n\t"
    "v_cvt_pk_bf16_f32 v227, v18, v19\n\t"
    "s_waitcnt vmcnt(14)\n\t"
    "v_mfma_f32_16x16x32_bf16 v[12:15], v[188:191], v[116:119], v[12:15]\n\t"
    "ds_write_b64 v30, v[226:227] offset:2048\n\t"
    "s_waitcnt vmcnt(13)\n\t"
    "v_mfma_f32_16x16x32_bf16 v[12:15], v[192:195], v[120:123], v[12:15]\n\t"
    "s_waitcnt vmcnt(12)\n\t"
    "v_mfma_f32_16x16x32_bf16 v[12:15], v[196:199], v[124:127], v[12:15]\n\t"
    "ds_read_b128 v[220:223], v29 offset:18432\n\t"
    "s_waitcnt vmcnt(11)\n\t"
    "v_mfma_f32_16x16x32_bf16 v[16:19], v[176:179], v[128:131], 0\n\t"
    "s_waitcnt vmcnt(10)\n\t"
    "v_mfma_f32_16x16x32_bf16 v[16:19], v[180:183], v[132:135], v[16:19]\n\t"
    "v_cvt_pk_bf16_f32 v224, v20, v21\n\t"
    "s_waitcnt vmcnt(9)\n\t"
    "v_mfma_f32_16x16x32_bf16 v[16:19], v[184:187], v[136:139], v[16:19]\n\t"
    "v_cvt_pk_bf16_f32 v225, v22, v23\n\t"
    "s_waitcnt vmcnt(8)\n\t"
    "v_mfma_f32_16x16x32_bf16 v[16:19], v[188:191], v[140:143], v[16:19]\n\t"
    "ds_write_b64 v30, v[224:225] offset:4096\n\t"
    "s_waitcnt vmcnt(7)\n\t"
    "v_mfma_f32_16x16x32_bf16 v[16:19], v[192:195], v[144:147], v[16:19]\n\t"
    "s_waitcnt vmcnt(6)\n\t"
    "v_mfma_f32_16x16x32_bf16 v[16:19], v[196:199], v[148:151], v[16:19]\n\t"
    "s_waitcnt vmcnt(5)\n\t"
    "v_mfma_f32_16x16x32_bf16 v[20:23], v[176:179], v[152:155], 0\n\t"
    "s_waitcnt vmcnt(4)\n\t"
    "v_mfma_f32_16x16x32_bf16 v[20:23], v[180:183], v[156:159], v[20:23]\n\t"
    "v_cvt_pk_bf16_f32 v226, v12, v13\n\t"
    "s_waitcnt vmcnt(3)\n\t"
    "v_mfma_f32_16x16x32_bf16 v[20:23], v[184:187], v[160:163], v[20:23]\n\t"
    "v_cvt_pk_bf16_f32 v227, v14, v15\n\t"
    "s_waitcnt vmcnt(2)\n\t"
    "v_mfma_f32_16x16x32_bf16 v[20:23], v[188:191], v[164:167], v[20:23]\n\t"
    "ds_write_b64 v30, v[226:227] offset:6144\n\t"
    "s_waitcnt vmcnt(1)\n\t"
    "v_mfma_f32_16x16x32_bf16 v[20:23], v[192:195], v[168:171], v[20:23]\n\t"
    "s_waitcnt vmcnt(0)\n\t"
    "v_mfma_f32_16x16x32_bf16 v[20:23], v[196:199], v[172:175], v[20:23]\n\t"
    "s_waitcnt lgkmcnt(2)\n\t"
    "v_mfma_f32_16x16x32_bf16 v[12:15], v[200:203], v[32:35], 0\n\t"
    "v_mfma_f32_16x16x32_bf16 v[12:15], v[204:207], v[36:39], v[12:15]\n\t"
    "v_cvt_pk_bf16_f32 v224, v16, v17\n\t"
    "v_mfma_f32_16x16x32_bf16 v[12:15], v[208:211], v[40:43], v[12:15]\n\t"
    "v_cvt_pk_bf16_f32 v225, v18, v19\n\t"
    "v_mfma_f32_16x16x32_bf16 v[12:15], v[212:215], v[44:47], v[12:15]\n\t"
    "ds_write_b64 v30, v[224:225] offset:8192\n\t"
    "v_mfma_f32_16x16x32_bf16 v[12:15], v[216:219], v[48:51], v[12:15]\n\t"
    "v_mfma_f32_16x16x32_bf16 v[12:15], v[220:223], v[52:55], v[12:15]\n\t"
    "v_mfma_f32_16x16x32_bf16 v[16:19], v[200:203], v[56:59], 0\n\t"
    "ds_read_b128 v[176:179], v28 offset:4096\n\t"
    "v_mfma_f32_16x16x32_bf16 v[16:19], v[204:207], v[60:63], v[16:19]\n\t"
    "v_cvt_pk_bf16_f32 v226, v20, v21\n\t"
    "v_mfma_f32_16x16x32_bf16 v[16:19], v[208:211], v[64:67], v[16:19]\n\t"
    "v_cvt_pk_bf16_f32 v227, v22, v23\n\t"
    "v_mfma_f32_16x16x32_bf16 v[16:19], v[212:215], v[68:71], v[16:19]\n\t"
    "ds_write_b64 v30, v[226:227] offset:10240\n\t"
    "v_mfma_f32_16x16x32_bf16 v[16:19], v[216:219], v[72:75], v[16:19]\n\t"
    "v_mfma_f32_16x16x32_bf16 v[16:19], v[220:223], v[76:79], v[16:19]\n\t"
    "ds_read_b128 v[180:183], v29 offset:4096\n\t"
    "v_mfma_f32_16x16x32_bf16 v[20:23], v[200:203], v[80:83], 0\n\t"
    "ds_read_b128 v[184:187], v28 offset:12288\n\t"
    "v_mfma_f32_16x16x32_bf16 v[20:23], v[204:207], v[84:87], v[20:23]\n\t"
    "v_cvt_pk_bf16_f32 v224, v12, v13\n\t"
    "v_mfma_f32_16x16x32_bf16 v[20:23], v[208:211], v[88:91], v[20:23]\n\t"
    "v_cvt_pk_bf16_f32 v225, v14, v15\n\t"
    "v_mfma_f32_16x16x32_bf16 v[20:23], v[212:215], v[92:95], v[20:23]\n\t"
    "ds_write_b64 v30, v[224:225] offset:8\n\t"
    "v_mfma_f32_16x16x32_bf16 v[20:23], v[216:219], v[96:99], v[20:23]\n\t"
    "v_mfma_f32_16x16x32_bf16 v[20:23], v[220:223], v[100:103], v[20:23]\n\t"
    "ds_read_b128 v[188:191], v29 offset:12288\n\t"
    "v_mfma_f32_16x16x32_bf16 v[12:15], v[200:203], v[104:107], 0\n\t"
    "ds_read_b128 v[192:195], v28 offset:20480\n\t"
    "v_mfma_f32_16x16x32_bf16 v[12:15], v[204:207], v[108:111], v[12:15]\n\t"
    "v_cvt_pk_bf16_f32 v226, v16, v17\n\t"
    "v_mfma_f32_16x16x32_bf16 v[12:15], v[208:211], v[112:115], v[12:15]\n\t"
    "v_cvt_pk_bf16_f32 v227, v18, v19\n\t"
    "v_mfma_f32_16x16x32_bf16 v[12:15], v[212:215], v[116:119], v[12:15]\n\t"
    "ds_write_b64 v30, v[226:227] offset:2056\n\t"
    "v_mfma_f32_16x16x32_bf16 v[12:15], v[216:219], v[120:123], v[12:15]\n\t"
    "v_mfma_f32_16x16x32_bf16 v[12:15], v[220:223], v[124:127], v[12:15]\n\t"
    "ds_read_b128 v[196:199], v29 offset:20480\n\t"
    "v_mfma_f32_16x16x32_bf16 v[16:19], v[200:203], v[128:131], 0\n\t"
    "v_mfma_f32_16x16x32_bf16 v[16:19], v[204:207], v[132:135], v[16:19]\n\t"
    "v_cvt_pk_bf16_f32 v224, v20, v21\n\t"
    "v_mfma_f32_16x16x32_bf16 v[16:19], v[208:211], v[136:139], v[16:19]\n\t"
    "v_cvt_pk_bf16_f32 v225, v22, v23\n\t"
    "v_mfma_f32_16x16x32_bf16 v[16:19], v[212:215], v[140:143], v[16:19]\n\t"
    "ds_write_b64 v30, v[224:225] offset:4104\n\t"
    "v_mfma_f32_16x16x32_bf16 v[16:19], v[216:219], v[144:147], v[16:19]\n\t"
    "v_mfma_f32_16x16x32_bf16 v[16:19], v[220:223], v[148:151], v[16:19]\n\t"
    "v_mfma_f32_16x16x32_bf16 v[20:23], v[200:203], v[152:155], 0\n\t"
    "v_mfma_f32_16x16x32_bf16 v[20:23], v[204:207], v[156:159], v[20:23]\n\t"
    "v_cvt_pk_bf16_f32 v226, v12, v13\n\t"
    "v_mfma_f32_16x16x32_bf16 v[20:23], v[208:211], v[160:163], v[20:23]\n\t"
    "v_cvt_pk_bf16_f32 v227, v14, v15\n\t"
    "v_mfma_f32_16x16x32_bf16 v[20:23], v[212:215], v[164:167], v[20:23]\n\t"
    "ds_write_b64 v30, v[226:227] offset:6152\n\t"
    "v_mfma_f32_16x16x32_bf16 v[20:23], v[216:219], v[168:171], v[20:23]\n\t"
    "v_mfma_f32_16x16x32_bf16 v[20:23], v[220:223], v[172:175], v[20:23]\n\t"
    "s_waitcnt lgkmcnt(2)\n\t"
    "v_mfma_f32_16x16x32_bf16 v[12:15], v[176:179], v[32:35], 0\n\t"
    "v_mfma_f32_16x16x32_bf16 v[12:15], v[180:183], v[36:39], v[12:15]\n\t"
    "v_cvt_pk_bf16_f32 v224, v16, v17\n\t"
    "v_mfma_f32_16x16x32_bf16 v[12:15], v[184:187], v[40:43], v[12:15]\n\t"
    "v_cvt_pk_bf16_f32 v225, v18, v19\n\t"
    "v_mfma_f32_16x16x32_bf16 v[12:15], v[188:191], v[44:47], v[12:15]\n\t"
    "ds_write_b64 v30, v[224:225] offset:8200\n\t"
    "v_mfma_f32_16x16x32_bf16 v[12:15], v[192:195], v[48:51], v[12:15]\n\t"
    "v_mfma_f32_16x16x32_bf16 v[12:15], v[196:199], v[52:55], v[12:15]\n\t"
    "v_mfma_f32_16x16x32_bf16 v[16:19], v[176:179], v[56:59], 0\n\t"
    "ds_read_b128 v[200:203], v28 offset:6144\n\t"
    "v_mfma_f32_16x16x32_bf16 v[16:19], v[180:183], v[60:63], v[16:19]\n\t"
    "v_cvt_pk_bf16_f32 v226, v20, v21\n\t"
    "v_mfma_f32_16x16x32_bf16 v[16:19], v[184:187], v[64:67], v[16:19]\n\t"
    "v_cvt_pk_bf16_f32 v227, v22, v23\n\t"
    "v_mfma_f32_16x16x32_bf16 v[16:19], v[188:191], v[68:71], v[16:19]\n\t"
    "ds_write_b64 v30, v[226:227] offset:10248\n\t"
    "v_mfma_f32_16x16x32_bf16 v[16:19], v[192:195], v[72:75], v[16:19]\n\t"
    "v_mfma_f32_16x16x32_bf16 v[16:19], v[196:199], v[76:79], v[16:19]\n\t"
    "ds_read_b128 v[204:207], v29 offset:6144\n\t"
    "v_mfma_f32_16x16x32_bf16 v[20:23], v[176:179], v[80:83], 0\n\t"
    "ds_read_b128 v[208:211], v28 offset:14336\n\t"
    "v_mfma_f32_16x16x32_bf16 v[20:23], v[180:183], v[84:87], v[20:23]\n\t"
    "v_cvt_pk_bf16_f32 v224, v12, v13\n\t"
    "v_mfma_f32_16x16x32_bf16 v[20:23], v[184:187], v[88:91], v[20:23]\n\t"
    "v_cvt_pk_bf16_f32 v225, v14, v15\n\t"
    "v_mfma_f32_16x16x32_bf16 v[20:23], v[188:191], v[92:95], v[20:23]\n\t"
    "ds_write_b64 v30, v[224:225] offset:1024\n\t"
    "v_mfma_f32_16x16x32_bf16 v[20:23], v[192:195], v[96:99], v[20:23]\n\t"
    "v_mfma_f32_16x16x32_bf16 v[20:23], v[196:199], v[100:103], v[20:23]\n\t"
    "ds_read_b128 v[212:215], v29 offset:14336\n\t"
    "v_mfma_f32_16x16x32_bf16 v[12:15], v[176:179], v[104:107], 0\n\t"
    "ds_read_b128 v[216:219], v28 offset:22528\n\t"
    "v_mfma_f32_16x16x32_bf16 v[12:15], v[180:183], v[108:111], v[12:15]\n\t"
    "v_cvt_pk_bf16_f32 v226, v16, v17\n\t"
    "v_mfma_f32_16x16x32_bf16 v[12:15], v[184:187], v[112:115], v[12:15]\n\t"
    "v_cvt_pk_bf16_f32 v227, v18, v19\n\t"
    "v_mfma_f32_16x16x32_bf16 v[12:15], v[188:191], v[116:119], v[12:15]\n\t"
    "ds_write_b64 v30, v[226:227] offset:3072\n\t"
    "v_mfma_f32_16x16x32_bf16 v[12:15], v[192:195], v[120:123], v[12:15]\n\t"
    "v_mfma_f32_16x16x32_bf16 v[12:15], v[196:199], v[124:127], v[12:15]\n\t"
    "ds_read_b128 v[220:223], v29 offset:22528\n\t"
    "v_mfma_f32_16x16x32_bf16 v[16:19], v[176:179], v[128:131], 0\n\t"
    "ds_read_b32 v250, %[fl4] offset:0\n\t"
    "v_mfma_f32_16x16x32_bf16 v[16:19], v[180:183], v[132:135], v[16:19]\n\t"
    "v_cvt_pk_bf16_f32 v224, v20, v21\n\t"
    "v_mfma_f32_16x16x32_bf16 v[16:19], v[184:187], v[136:139], v[16:19]\n\t"
    "v_cvt_pk_bf16_f32 v225, v22, v23\n\t"
    "v_mfma_f32_16x16x32_bf16 v[16:19], v[188:191], v[140:143], v[16:19]\n\t"
    "ds_write_b64 v30, v[224:225] offset:5120\n\t"
    "v_mfma_f32_16x16x32_bf16 v[16:19], v[192:195], v[144:147], v[16:19]\n\t"
    "v_mfma_f32_16x16x32_bf16 v[16:19], v[196:199], v[148:151], v[16:19]\n\t"
    "v_mfma_f32_16x16x32_bf16 v[20:23], v[176:179], v[152:155], 0\n\t"
    "s_cmp_eq_u32 s63, 2\n\t"
    "s_cselect_b32 s60, 4294918144, s64\n\t"
    "v_mfma_f32_16x16x32_bf16 v[20:23], v[180:183], v[156:159], v[20:23]\n\t"
    "v_cvt_pk_bf16_f32 v226, v12, v13\n\t"
    "v_mfma_f32_16x16x32_bf16 v[20:23], v[184:187], v[160:163], v[20:23]\n\t"
    "v_cvt_pk_bf16_f32 v227, v14, v15\n\t"
    "v_mfma_f32_16x16x32_bf16 v[20:23], v[188:191], v[164:167], v[20:23]\n\t"
    "ds_write_b64 v30, v[226:227] offset:7168\n\t"
    "v_mfma_f32_16x16x32_bf16 v[20:23], v[192:195], v[168:171], v[20:23]\n\t"
    "s_cselect_b32 s63, -1, s63\n\t"
    "v_mfma_f32_16x16x32_bf16 v[20:23], v[196:199], v[172:175], v[20:23]\n\t"
    "s_waitcnt lgkmcnt(2)\n\t"
    "v_cmp_gt_u32 vcc, s62, v250\n\t"
    "s_cbranch_vccnz .Lsv3%=\n\t"
    ".Lrv3%=:\n\t"
    "v_mfma_f32_16x16x32_bf16 v[12:15], v[200:203], v[32:35], 0\n\t"
    "v_add_u32 v28, s60, v28\n\t"
    "v_mfma_f32_16x16x32_bf16 v[12:15], v[204:207], v[36:39], v[12:15]\n\t"
    "v_cvt_pk_bf16_f32 v224, v16, v17\n\t"
    "v_mfma_f32_16x16x32_bf16 v[12:15], v[208:211], v[40:43], v[12:15]\n\t"
    "v_cvt_pk_bf16_f32 v225, v18, v19\n\t"
    "v_mfma_f32_16x16x32_bf16 v[12:15], v[212:215], v[44:47], v[12:15]\n\t"
    "ds_write_b64 v30, v[224:225] offset:9216\n\t"
    "v_mfma_f32_16x16x32_bf16 v[12:15], v[216:219], v[48:51], v[12:15]\n\t"
    "v_add_u32 v29, s60, v29\n\t"
    "v_mfma_f32_16x16x32_bf16 v[12:15], v[220:223], v[52:55], v[12:15]\n\t"
    "s_add_u32 s63, s63, 1\n\t"
    "v_mfma_f32_16x16x32_bf16 v[16:19], v[200:203], v[56:59], 0\n\t"
    "ds_read_b128 v[176:179], v28 offset:0\n\t"
    "v_mfma_f32_16x16x32_bf16 v[16:19], v[204:207], v[60:63], v[16:19]\n\t"
    "v_cvt_pk_bf16_f32 v226, v20, v21\n\t"
    "v_mfma_f32_16x16x32_bf16 v[16:19], v[208:211], v[64:67], v[16:19]\n\t"
    "v_cvt_pk_bf16_f32 v227, v22, v23\n\t"
    "v_mfma_f32_16x16x32_bf16 v[16:19], v[212:215], v[68:71], v[16:19]\n\t"
    "ds_write_b64 v30, v[226:227] offset:11264\n\t"
    "v_mfma_f32_16x16x32_bf16 v[16:19], v[216:219], v[72:75], v[16:19]\n\t"
    "v_mfma_f32_16x16x32_bf16 v[16:19], v[220:223], v[76:79], v[16:19]\n\t"
    "ds_read_b128 v[180:183], v29 offset:0\n\t"
    "v_mfma_f32_16x16x32_bf16 v[20:23], v[200:203], v[80:83], 0\n\t"
    "ds_read_b128 v[184:187], v28 offset:8192\n\t"
    "v_mfma_f32_16x16x32_bf16 v[20:23], v[204:207], v[84:87], v[20:23]\n\t"
    "v_cvt_pk_bf16_f32 v224, v12, v13\n\t"
    "v_mfma_f32_16x16x32_bf16 v[20:23], v[208:211], v[88:91], v[20:23]\n\t"
    "v_cvt_pk_bf16_f32 v225, v14, v15\n\t"
    "v_mfma_f32_16x16x32_bf16 v[20:23], v[212:215], v[92:95], v[20:23]\n\t"
    "ds_write_b64 v30, v[224:225] offset:1032\n\t"
    "v_mfma_f32_16x16x32_bf16 v[20:23], v[216:219], v[96:99], v[20:23]\n\t"
    "v_mfma_f32_16x16x32_bf16 v[20:23], v[220:223], v[100:103], v[20:23]\n\t"
    "ds_read_b128 v[188:191], v29 offset:8192\n\t"
    "v_mfma_f32_16x16x32_bf16 v[12:15], v[200:203], v[104:107], 0\n\t"
    "ds_read_b128 v[192:195], v28 offset:16384\n\t"
    "v_mfma_f32_16x16x32_bf16 v[12:15], v[204:207], v[108:111], v[12:15]\n\t"
    "v_cvt_pk_bf16_f32 v226, v16, v17\n\t"
    "v_mfma_f32_16x16x32_bf16 v[12:15], v[208:211], v[112:115], v[12:15]\n\t"
    "v_cvt_pk_bf16_f32 v227, v18, v19\n\t"
    "v_mfma_f32_16x16x32_bf16 v[12:15], v[212:215], v[116:119], v[12:15]\n\t"
    "ds_write_b64 v30, v[226:227] offset:3080\n\t"
    "v_mfma_f32_16x16x32_bf16 v[12:15], v[216:219], v[120:123], v[12:15]\n\t"
    "v_mfma_f32_16x16x32_bf16 v[12:15], v[220:223], v[124:127], v[12:15]\n\t"
    "ds_read_b128 v[196:199], v29 offset:16384\n\t"
    "v_mfma_f32_16x16x32_bf16 v[16:19], v[200:203], v[128:131], 0\n\t"
    "v_mfma_f32_16x16x32_bf16 v[16:19], v[204:207], v[132:135], v[16:19]\n\t"
    "v_cvt_pk_bf16_f32 v224, v20, v21\n\t"
    "v_mfma_f32_16x16x32_bf16 v[16:19], v[208:211], v[136:139], v[16:19]\n\t"
    "v_cvt_pk_bf16_f32 v225, v22, v23\n\t"
    "v_mfma_f32_16x16x32_bf16 v[16:19], v[212:215], v[140:143], v[16:19]\n\t"
    "ds_write_b64 v30, v[224:225] offset:5128\n\t"
    "v_mfma_f32_16x16x32_bf16 v[16:19], v[216:219], v[144:147], v[16:19]\n\t"
    "v_mfma_f32_16x16x32_bf16 v[16:19], v[220:223], v[148:151], v[16:19]\n\t"
    "v_mfma_f32_16x16x32_bf16 v[20:23], v[200:203], v[152:155], 0\n\t"
    "s_add_u32 s61, s61, 1\n\t"
    "s_add_u32 s62, s62, 1\n\t"
    "v_mfma_f32_16x16x32_bf16 v[20:23], v[204:207], v[156:159], v[20:23]\n\t"
    "v_cvt_pk_bf16_f32 v226, v12, v13\n\t"
    "v_mfma_f32_16x16x32_bf16 v[20:23], v[208:211], v[160:163], v[20:23]\n\t"
    "v_cvt_pk_bf16_f32 v227, v14, v15\n\t"
    "v_mfma_f32_16x16x32_bf16 v[20:23], v[212:215], v[164:167], v[20:23]\n\t"
    "ds_write_b64 v30, v[226:227] offset:7176\n\t"
    "v_mfma_f32_16x16x32_bf16 v[20:23], v[216:219], v[168:171], v[20:23]\n\t"
    "v_swap_b32 v30, v31\n\t"
    "v_mfma_f32_16x16x32_bf16 v[20:23], v[220:223], v[172:175], v[20:23]\n\t"
    "s_cmp_le_u32 s61, 9\n\t"
    ".Lloopv%=:\n\t"
    "s_waitcnt lgkmcnt(2)\n\t"
    "v_mfma_f32_16x16x32_bf16 v[12:15], v[176:179], v[32:35], 0\n\t"
    "v_mfma_f32_16x16x32_bf16 v[12:15], v[180:183], v[36:39], v[12:15]\n\t"
    "v_cvt_pk_bf16_f32 v224, v16, v17\n\t"
    "v_mfma_f32_16x16x32_bf16 v[12:15], v[184:187], v[40:43], v[12:15]\n\t"
    "v_cvt_pk_bf16_f32 v225, v18, v19\n\t"
    "v_mfma_f32_16x16x32_bf16 v[12:15], v[188:191], v[44:47], v[12:15]\n\t"
    "ds_write_b64 v31, v[224:225] offset:9224\n\t"
    "v_mfma_f32_16x16x32_bf16 v[12:15], v[192:195], v[48:51], v[12:15]\n\t"
    "ds_read_b32 v249, %[fl4] offset:32\n\t"
    "v_mfma_f32_16x16x32_bf16 v[12:15], v[196:199], v[52:55], v[12:15]\n\t"
    "v_mfma_f32_16x16x32_bf16 v[16:19], v[176:179], v[56:59], 0\n\t"
    "ds_read_b128 v[200:203], v28 offset:2048\n\t"
    "v_mfma_f32_16x16x32_bf16 v[16:19], v[180:183], v[60:63], v[16:19]\n\t"
    "v_cvt_pk_bf16_f32 v226, v20, v21\n\t"
    "v_mfma_f32_16x16x32_bf16 v[16:19], v[184:187], v[64:67], v[16:19]\n\t"
    "v_cvt_pk_bf16_f32 v227, v22, v23\n\t"
    "v_mfma_f32_16x16x32_bf16 v[16:19], v[188:191], v[68:71], v[16:19]\n\t"
    "ds_write_b64 v31, v[226:227] offset:11272\n\t"
    "v_mfma_f32_16x16x32_bf16 v[16:19], v[192:195], v[72:75], v[16:19]\n\t"
    "ds_write_b32 %[own], v248 offset:48\n\t"
    "v_mfma_f32_16x16x32_bf16 v[16:19], v[196:199], v[76:79], v[16:19]\n\t"
    "ds_read_b128 v[204:207], v29 offset:2048\n\t"
    "v_add_u32 v248, 1, v248\n\t"
    "s_waitcnt lgkmcnt(4)\n\t"
    "v_cmp_gt_u32 vcc, s61, v249\n\t"
    "s_cbranch_vccnz .Lsv4%=\n\t"
    ".Lrv4%=:\n\t"
    "v_mfma_f32_16x16x32_bf16 v[20:23], v[176:179], v[80:83], 0\n\t"
    "ds_read_b128 v[208:211], v28 offset:10240\n\t"
    "v_mfma_f32_16x16x32_bf16 v[20:23], v[180:183], v[84:87], v[20:23]\n\t"
    "v_cvt_pk_bf16_f32 v224, v12, v13\n\t"
    "v_mfma_f32_16x16x32_bf16 v[20:23], v[184:187], v[88:91], v[20:23]\n\t"
    "v_cvt_pk_bf16_f32 v225, v14, v15\n\t"
    "v_mfma_f32_16x16x32_bf16 v[20:23], v[188:191], v[92:95], v[20:23]\n\t"
    "ds_write_b64 v30, v[224:225] offset:0\n\t"
    "v_mfma_f32_16x16x32_bf16 v[20:23], v[192:195], v[96:99], v[20:23]\n\t"
    "v_mfma_f32_16x16x32_bf16 v[20:23], v[196:199], v[100:103], v[20:23]\n\t"
    "ds_read_b128 v[212:215], v29 offset:10240\n\t"
    "v_mfma_f32_16x16x32_bf16 v[12:15], v[176:179], v[104:107], 0\n\t"
    "ds_read_b128 v[216:219], v28 offset:18432\n\t"
    "v_mfma_f32_16x16x32_bf16 v[12:15], v[180:183], v[108:111], v[12:15]\n\t"
    "v_cvt_pk_bf16_f32 v226, v16, v17\n\t"
    "v_mfma_f32_16x16x32_bf16 v[12:15], v[184:187], v[112:115], v[12:15]\n\t"
    "v_cvt_pk_bf16_f32 v227, v18, v19\n\t"
    "v_mfma_f32_16x16x32_bf16 v[12:15], v[188:191], v[116:119], v[12:15]\n\t"
    "ds_write_b64 v30, v[226:227] offset:2048\n\t"
    "v_mfma_f32_16x16x32_bf16 v[12:15], v[192:195], v[120:123], v[12:15]\n\t"
    "v_mfma_f32_16x16x32_bf16 v[12:15], v[196:199], v[124:127], v[12:15]\n\t"
    "ds_read_b128 v[220:223], v29 offset:18432\n\t"
    "v_mfma_f32_16x16x32_bf16 v[16:19], v[176:179], v[128:131], 0\n\t"
    "v_mfma_f32_16x16x32_bf16 v[16:19], v[180:183], v[132:135], v[16:19]\n\t"
    "v_cvt_pk_bf16_f32 v224, v20, v21\n\t"
    "v_mfma_f32_16x16x32_bf16 v[16:19], v[184:187], v[136:139], v[16:19]\n\t"
    "v_cvt_pk_bf16_f32 v225, v22, v23\n\t"
    "v_mfma_f32_16x16x32_bf16 v[16:19], v[188:191], v[140:143], v[16:19]\n\t"
    "ds_write_b64 v30, v[224:225] offset:4096\n\t"
    "v_mfma_f32_16x16x32_bf16 v[16:19], v[192:195], v[144:147], v[16:19]\n\t"
    "v_mfma_f32_16x16x32_bf16 v[16:19], v[196:199], v[148:151], v[16:19]\n\t"
    "v_mfma_f32_16x16x32_bf16 v[20:23], v[176:179], v[152:155], 0\n\t"
    "v_mfma_f32_16x16x32_bf16 v[20:23], v[180:183], v[156:159], v[20:23]\n\t"
    "v_cvt_pk_bf16_f32 v226, v12, v13\n\t"
    "v_mfma_f32_16x16x32_bf16 v[20:23], v[184:187], v[160:163], v[20:23]\n\t"
    "v_cvt_pk_bf16_f32 v227, v14, v15\n\t"
    "v_mfma_f32_16x16x32_bf16 v[20:23], v[188:191], v[164:167], v[20:23]\n\t"
    "ds_write_b64 v30, v[226:227] offset:6144\n\t"
    "v_mfma_f32_16x16x32_bf16 v[20:23], v[192:195], v[168:171], v[20:23]\n\t"
    "v_mfma_f32_16x16x32_bf16 v[20:23], v[196:199], v[172:175], v[20:23]\n\t"
    "s_waitcnt lgkmcnt(2)\n\t"
    "v_mfma_f32_16x16x32_bf16 v[12:15], v[200:203], v[32:35], 0\n\t"
    "v_mfma_f32_16x16x32_bf16 v[12:15], v[204:207], v[36:39], v[12:15]\n\t"
    "v_cvt_pk_bf16_f32 v224, v16, v17\n\t"
    "v_mfma_f32_16x16x32_bf16 v[12:15], v[208:211], v[40:43], v[12:15]\n\t"
    "v_cvt_pk_bf16_f32 v225, v18, v19\n\t"
    "v_mfma_f32_16x16x32_bf16 v[12:15], v[212:215], v[44:47], v[12:15]\n\t"
    "ds_write_b64 v30, v[224:225] offset:8192\n\t"
    "v_mfma_f32_16x16x32_bf16 v[12:15], v[216:219], v[48:51], v[12:15]\n\t"
    "v_mfma_f32_16x16x32_bf16 v[12:15], v[220:223], v[52:55], v[12:15]\n\t"
    "v_mfma_f32_16x16x32_bf16 v[16:19], v[200:203], v[56:59], 0\n\t"
    "ds_read_b128 v[176:179], v28 offset:4096\n\t"
    "v_mfma_f32_16x16x32_bf16 v[16:19], v[204:207], v[60:63], v[16:19]\n\t"
    "v_cvt_pk_bf16_f32 v226, v20, v21\n\t"
    "v_mfma_f32_16x16x32_bf16 v[16:19], v[208:211], v[64:67], v[16:19]\n\t"
    "v_cvt_pk_bf16_f32 v227, v22, v23\n\t"
    "v_mfma_f32_16x16x32_bf16 v[16:19], v[212:215], v[68:71], v[16:19]\n\t"
    "ds_write_b64 v30, v[226:227] offset:10240\n\t"
    "v_mfma_f32_16x16x32_bf16 v[16:19], v[216:219], v[72:75], v[16:19]\n\t"
    "v_mfma_f32_16x16x32_bf16 v[16:19], v[220:223], v[76:79], v[16:19]\n\t"
    "ds_read_b128 v[180:183], v29 offset:4096\n\t"
    "v_mfma_f32_16x16x32_bf16 v[20:23], v[200:203], v[80:83], 0\n\t"
    "ds_read_b128 v[184:187], v28 offset:12288\n\t"
    "v_mfma_f32_16x16x32_bf16 v[20:23], v[204:207], v[84:87], v[20:23]\n\t"
    "v_cvt_pk_bf16_f32 v224, v12, v13\n\t"
    "v_mfma_f32_16x16x32_bf16 v[20:23], v[208:211], v[88:91], v[20:23]\n\t"
    "v_cvt_pk_bf16_f32 v225, v14, v15\n\t"
    "v_mfma_f32_16x16x32_bf16 v[20:23], v[212:215], v[92:95], v[20:23]\n\t"
    "ds_write_b64 v30, v[224:225] offset:8\n\t"
    "v_mfma_f32_16x16x32_bf16 v[20:23], v[216:219], v[96:99], v[20:23]\n\t"
    "v_mfma_f32_16x16x32_bf16 v[20:23], v[220:223], v[100:103], v[20:23]\n\t"
    "ds_read_b128 v[188:191], v29 offset:12288\n\t"
    "v_mfma_f32_16x16x32_bf16 v[12:15], v[200:203], v[104:107], 0\n\t"
    "ds_read_b128 v[192:195], v28 offset:20480\n\t"
    "v_mfma_f32_16x16x32_bf16 v[12:15], v[204:207], v[108:111], v[12:15]\n\t"
    "v_cvt_pk_bf16_f32 v226, v16, v17\n\t"
    "v_mfma_f32_16x16x32_bf16 v[12:15], v[208:211], v[112:115], v[12:15]\n\t"
    "v_cvt_pk_bf16_f32 v227, v18, v19\n\t"
    "v_mfma_f32_16x16x32_bf16 v[12:15], v[212:215], v[116:119], v[12:15]\n\t"
    "ds_write_b64 v30, v[226:227] offset:2056\n\t"
    "v_mfma_f32_16x16x32_bf16 v[12:15], v[216:219], v[120:123], v[12:15]\n\t"
    "v_mfma_f32_16x16x32_bf16 v[12:15], v[220:223], v[124:127], v[12:15]\n\t"
    "ds_read_b128 v[196:199], v29 offset:20480\n\t"
    "v_mfma_f32_16x16x32_bf16 v[16:19], v[200:203], v[128:131], 0\n\t"
    "v_mfma_f32_16x16x32_bf16 v[16:19], v[204:207], v[132:135], v[16:19]\n\t"
    "v_cvt_pk_bf16_f32 v224, v20, v21\n\t"
    "v_mfma_f32_16x16x32_bf16 v[16:19], v[208:211], v[136:139], v[16:19]\n\t"
    "v_cvt_pk_bf16_f32 v225, v22, v23\n\t"
    "v_mfma_f32_16x16x32_bf16 v[16:19], v[212:215], v[140:143], v[16:19]\n\t"
    "ds_write_b64 v30, v[224:225] offset:4104\n\t"
    "v_mfma_f32_16x16x32_bf16 v[16:19], v[216:219], v[144:147], v[16:19]\n\t"
    "v_mfma_f32_16x16x32_bf16 v[16:19], v[220:223], v[148:151], v[16:19]\n\t"
    "v_mfma_f32_16x16x32_bf16 v[20:23], v[200:203], v[152:155], 0\n\t"
    "v_mfma_f32_16x16x32_bf16 v[20:23], v[204:207], v[156:159], v[20:23]\n\t"
    "v_cvt_pk_bf16_f32 v226, v12, v13\n\t"
    "v_mfma_f32_16x16x32_bf16 v[20:23], v[208:211], v[160:163], v[20:23]\n\t"
    "v_cvt_pk_bf16_f32 v227, v14, v15\n\t"
    "v_mfma_f32_16x16x32_bf16 v[20:23], v[212:215], v[164:167], v[20:23]\n\t"
    "ds_write_b64 v30, v[226:227] offset:6152\n\t"
    "v_mfma_f32_16x16x32_bf16 v[20:23], v[216:219], v[168:171], v[20:23]\n\t"
    "v_mfma_f32_16x16x32_bf16 v[20:23], v[220:223], v[172:175], v[20:23]\n\t"
    "s_waitcnt lgkmcnt(2)\n\t"
    "v_mfma_f32_16x16x32_bf16 v[12:15], v[176:179], v[32:35], 0\n\t"
    "v_mfma_f32_16x16x32_bf16 v[12:15], v[180:183], v[36:39], v[12:15]\n\t"
    "v_cvt_pk_bf16_f32 v224, v16, v17\n\t"
    "v_mfma_f32_16x16x32_bf16 v[12:15], v[184:187], v[40:43], v[12:15]\n\t"
    "v_cvt_pk_bf16_f32 v225, v18, v19\n\t"
    "v_mfma_f32_16x16x32_bf16 v[12:15], v[188:191], v[44:47], v[12:15]\n\t"
    "ds_write_b64 v30, v[224:225] offset:8200\n\t"
    "v_mfma_f32_16x16x32_bf16 v[12:15], v[192:195], v[48:51], v[12:15]\n\t"
    "v_mfma_f32_16x16x32_bf16 v[12:15], v[196:199], v[52:55], v[12:15]\n\t"
    "v_mfma_f32_16x16x32_bf16 v[16:19], v[176:179], v[56:59], 0\n\t"
    "ds_read_b128 v[200:203], v28 offset:6144\n\t"
    "v_mfma_f32_16x16x32_bf16 v[16:19], v[180:183], v[60:63], v[16:19]\n\t"
    "v_cvt_pk_bf16_f32 v226, v20, v21\n\t"
    "v_mfma_f32_16x16x32_bf16 v[16:19], v[184:187], v[64:67], v[16:19]\n\t"
    "v_cvt_pk_bf16_f32 v227, v22, v23\n\t"
    "v_mfma_f32_16x16x32_bf16 v[16:19], v[188:191], v[68:71], v[16:19]\n\t"
    "ds_write_b64 v30, v[226:227] offset:10248\n\t"
    "v_mfma_f32_16x16x32_bf16 v[16:19], v[192:195], v[72:75], v[16:19]\n\t"
    "v_mfma_f32_16x16x32_bf16 v[16:19], v[196:199], v[76:79], v[16:19]\n\t"
    "ds_read_b128 v[204:207], v29 offset:6144\n\t"
    "v_mfma_f32_16x16x32_bf16 v[20:23], v[176:179], v[80:83], 0\n\t"
    "ds_read_b128 v[208:211], v28 offset:14336\n\t"
    "v_mfma_f32_16x16x32_bf16 v[20:23], v[180:183], v[84:87], v[20:23]\n\t"
    "v_cvt_pk_bf16_f32 v224, v12, v13\n\t"
    "v_mfma_f32_16x16x32_bf16 v[20:23], v[184:187], v[88:91], v[20:23]\n\t"
    "v_cvt_pk_bf16_f32 v225, v14, v15\n\t"
    "v_mfma_f32_16x16x32_bf16 v[20:23], v[188:191], v[92:95], v[20:23]\n\t"
    "ds_write_b64 v30, v[224:225] offset:1024\n\t"
    "v_mfma_f32_16x16x32_bf16 v[20:23], v[192:195], v[96:99], v[20:23]\n\t"
    "v_mfma_f32_16x16x32_bf16 v[20:23], v[196:199], v[100:103], v[20:23]\n\t"
    "ds_read_b128 v[212:215], v29 offset:14336\n\t"
    "v_mfma_f32_16x16x32_bf16 v[12:15], v[176:179], v[104:107], 0\n\t"
    "ds_read_b128 v[216:219], v28 offset:22528\n\t"
    "v_mfma_f32_16x16x32_bf16 v[12:15], v[180:183], v[108:111], v[12:15]\n\t"
    "v_cvt_pk_bf16_f32 v226, v16, v17\n\t"
    "v_mfma_f32_16x16x32_bf16 v[12:15], v[184:187], v[112:115], v[12:15]\n\t"
    "v_cvt_pk_bf16_f32 v227, v18, v19\n\t"
    "v_mfma_f32_16x16x32_bf16 v[12:15], v[188:191], v[116:119], v[12:15]\n\t"
    "ds_write_b64 v30, v[226:227] offset:3072\n\t"
    "v_mfma_f32_16x16x32_bf16 v[12:15], v[192:195], v[120:123], v[12:15]\n\t"
    "v_mfma_f32_16x16x32_bf16 v[12:15], v[196:199], v[124:127], v[12:15]\n\t"
    "ds_read_b128 v[220:223], v29 offset:22528\n\t"
    "v_mfma_f32_16x16x32_bf16 v[16:19], v[176:179], v[128:131], 0\n\t"
    "ds_read_b32 v250, %[fl4] offset:0\n\t"
    "v_mfma_f32_16x16x32_bf16 v[16:19], v[180:183], v[132:135], v[16:19]\n\t"
    "v_cvt_pk_bf16_f32 v224, v20, v21\n\t"
    "v_mfma_f32_16x16x32_bf16 v[16:19], v[184:187], v[136:139], v[16:19]\n\t"
    "v_cvt_pk_bf16_f32 v225, v22, v23\n\t"
    "v_mfma_f32_16x16x32_bf16 v[16:19], v[188:191], v[140:143], v[16:19]\n\t"
    "ds_write_b64 v30, v[224:225] offset:5120\n\t"
    "v_mfma_f32_16x16x32_bf16 v[16:19], v[192:195], v[144:147], v[16:19]\n\t"
    "v_mfma_f32_16x16x32_bf16 v[16:19], v[196:199], v[148:151], v[16:19]\n\t"
    "v_mfma_f32_16x16x32_bf16 v[20:23], v[176:179], v[152:155], 0\n\t"
    "s_cmp_eq_u32 s63, 2\n\t"
    "s_cselect_b32 s60, 4294918144, s64\n\t"
    "v_mfma_f32_16x16x32_bf16 v[20:23], v[180:183], v[156:159], v[20:23]\n\t"
    "v_cvt_pk_bf16_f32 v226, v12, v13\n\t"
    "v_mfma_f32_16x16x32_bf16 v[20:23], v[184:187], v[160:163], v[20:23]\n\t"
    "v_cvt_pk_bf16_f32 v227, v14, v15\n\t"
    "v_mfma_f32_16x16x32_bf16 v[20:23], v[188:191], v[164:167], v[20:23]\n\t"
    "ds_write_b64 v30, v[226:227] offset:7168\n\t"
    "v_mfma_f32_16x16x32_bf16 v[20:23], v[192:195], v[168:171], v[20:23]\n\t"
    "s_cselect_b32 s63, -1, s63\n\t"
    "v_mfma_f32_16x16x32_bf16 v[20:23], v[196:199], v[172:175], v[20:23]\n\t"
    "s_waitcnt lgkmcnt(2)\n\t"
    "v_cmp_gt_u32 vcc, s62, v250\n\t"
    "s_cbranch_vccnz .Lsv5%=\n\t"
    ".Lrv5%=:\n\t"
    "v_mfma_f32_16x16x32_bf16 v[12:15], v[200:203], v[32:35], 0\n\t"
    "v_add_u32 v28, s60, v28\n\t"
    "v_mfma_f32_16x16x32_bf16 v[12:15], v[204:207], v[36:39], v[12:15]\n\t"
    "v_cvt_pk_bf16_f32 v224, v16, v17\n\t"
    "v_mfma_f32_16x16x32_bf16 v[12:15], v[208:211], v[40:43], v[12:15]\n\t"
    "v_cvt_pk_bf16_f32 v225, v18, v19\n\t"
    "v_mfma_f32_16x16x32_bf16 v[12:15], v[212:215], v[44:47], v[12:15]\n\t"
    "ds_write_b64 v30, v[224:225] offset:9216\n\t"
    "v_mfma_f32_16x16x32_bf16 v[12:15], v[216:219], v[48:51], v[12:15]\n\t"
    "v_add_u32 v29, s60, v29\n\t"
    "v_mfma_f32_16x16x32_bf16 v[12:15], v[220:223], v[52:55], v[12:15]\n\t"
    "s_add_u32 s63, s63, 1\n\t"
    "v_mfma_f32_16x16x32_bf16 v[16:19], v[200:203], v[56:59], 0\n\t"
    "ds_read_b128 v[176:179], v28 offset:0\n\t"
    "v_mfma_f32_16x16x32_bf16 v[16:19], v[204:207], v[60:63], v[16:19]\n\t"
    "v_cvt_pk_bf16_f32 v226, v20, v21\n\t"
    "v_mfma_f32_16x16x32_bf16 v[16:19], v[208:211], v[64:67], v[16:19]\n\t"
    "v_cvt_pk_bf16_f32 v227, v22, v23\n\t"
    "v_mfma_f32_16x16x32_bf16 v[16:19], v[212:215], v[68:71], v[16:19]\n\t"
    "ds_write_b64 v30, v[226:227] offset:11264\n\t"
    "v_mfma_f32_16x16x32_bf16 v[16:19], v[216:219], v[72:75], v[16:19]\n\t"
    "v_mfma_f32_16x16x32_bf16 v[16:19], v[220:223], v[76:79], v[16:19]\n\t"
    "ds_read_b128 v[180:183], v29 offset:0\n\t"
    "v_mfma_f32_16x16x32_bf16 v[20:23], v[200:203], v[80:83], 0\n\t"
    "ds_read_b128 v[184:187], v28 offset:8192\n\t"
    "v_mfma_f32_16x16x32_bf16 v[20:23], v[204:207], v[84:87], v[20:23]\n\t"
    "v_cvt_pk_bf16_f32 v224, v12, v13\n\t"
    "v_mfma_f32_16x16x32_bf16 v[20:23], v[208:211], v[88:91], v[20:23]\n\t"
    "v_cvt_pk_bf16_f32 v225, v14, v15\n\t"
    "v_mfma_f32_16x16x32_bf16 v[20:23], v[212:215], v[92:95], v[20:23]\n\t"
    "ds_write_b64 v30, v[224:225] offset:1032\n\t"
    "v_mfma_f32_16x16x32_bf16 v[20:23], v[216:219], v[96:99], v[20:23]\n\t"
    "v_mfma_f32_16x16x32_bf16 v[20:23], v[220:223], v[100:103], v[20:23]\n\t"
    "ds_read_b128 v[188:191], v29 offset:8192\n\t"
    "v_mfma_f32_16x16x32_bf16 v[12:15], v[200:203], v[104:107], 0\n\t"
    "ds_read_b128 v[192:195], v28 offset:16384\n\t"
    "v_mfma_f32_16x16x32_bf16 v[12:15], v[204:207], v[108:111], v[12:15]\n\t"
    "v_cvt_pk_bf16_f32 v226, v16, v17\n\t"
    "v_mfma_f32_16x16x32_bf16 v[12:15], v[208:211], v[112:115], v[12:15]\n\t"
    "v_cvt_pk_bf16_f32 v227, v18, v19\n\t"
    "v_mfma_f32_16x16x32_bf16 v[12:15], v[212:215], v[116:119], v[12:15]\n\t"
    "ds_write_b64 v30, v[226:227] offset:3080\n\t"
    "v_mfma_f32_16x16x32_bf16 v[12:15], v[216:219], v[120:123], v[12:15]\n\t"
    "v_mfma_f32_16x16x32_bf16 v[12:15], v[220:223], v[124:127], v[12:15]\n\t"
    "ds_read_b128 v[196:199], v29 offset:16384\n\t"
    "v_mfma_f32_16x16x32_bf16 v[16:19], v[200:203], v[128:131], 0\n\t"
    "v_mfma_f32_16x16x32_bf16 v[16:19], v[204:207], v[132:135], v[16:19]\n\t"
    "v_cvt_pk_bf16_f32 v224, v20, v21\n\t"
    "v_mfma_f32_16x16x32_bf16 v[16:19], v[208:211], v[136:139], v[16:19]\n\t"
    "v_cvt_pk_bf16_f32 v225, v22, v23\n\t"
    "v_mfma_f32_16x16x32_bf16 v[16:19], v[212:215], v[140:143], v[16:19]\n\t"
    "ds_write_b64 v30, v[224:225] offset:5128\n\t"
    "v_mfma_f32_16x16x32_bf16 v[16:19], v[216:219], v[144:147], v[16:19]\n\t"
    "v_mfma_f32_16x16x32_bf16 v[16:19], v[220:223], v[148:151], v[16:19]\n\t"
    "v_mfma_f32_16x16x32_bf16 v[20:23], v[200:203], v[152:155], 0\n\t"
    "s_add_u32 s61, s61, 1\n\t"
    "s_add_u32 s62, s62, 1\n\t"
    "v_mfma_f32_16x16x32_bf16 v[20:23], v[204:207], v[156:159], v[20:23]\n\t"
    "v_cvt_pk_bf16_f32 v226, v12, v13\n\t"
    "v_mfma_f32_16x16x32_bf16 v[20:23], v[208:211], v[160:163], v[20:23]\n\t"
    "v_cvt_pk_bf16_f32 v227, v14, v15\n\t"
    "v_mfma_f32_16x16x32_bf16 v[20:23], v[212:215], v[164:167], v[20:23]\n\t"
    "ds_write_b64 v30, v[226:227] offset:7176\n\t"
    "v_mfma_f32_16x16x32_bf16 v[20:23], v[216:219], v[168:171], v[20:23]\n\t"
    "v_swap_b32 v30, v31\n\t"
    "v_mfma_f32_16x16x32_bf16 v[20:23], v[220:223], v[172:175], v[20:23]\n\t"
    "s_cmp_le_u32 s61, 9\n\t"
    "s_cbranch_scc1 .Lloopv%=\n\t"
    "v_cvt_pk_bf16_f32 v224, v16, v17\n\t"
    "v_cvt_pk_bf16_f32 v225, v18, v19\n\t"
    "ds_write_b64 v31, v[224:225] offset:9224\n\t"
    "s_nop 2\n\t"
    "v_cvt_pk_bf16_f32 v226, v20, v21\n\t"
    "v_cvt_pk_bf16_f32 v227, v22, v23\n\t"
    "ds_write_b64 v31, v[226:227] offset:11272\n\t"
    "ds_write_b32 %[own], v248 offset:48\n\t"
    "v_add_u32 v248, 1, v248\n\t"
    "s_branch .Lendv%=\n\t"
    ".Lsv1%=:\n\t"
    "s_sleep 1\n\t"
    "ds_read_b32 v249, %[fl4] offset:0\n\t"
    "s_waitcnt lgkmcnt(0)\n\t"
    "v_cmp_gt_u32 vcc, s61, v249\n\t"
    "s_cbranch_vccnz .Lsv1%=\n\t"
    "s_branch .Lrv1%=\n\t"
    ".Lsv2%=:\n\t"
    "s_sleep 1\n\t"
    "ds_read_b32 v249, %[fl4] offset:32\n\t"
    "s_waitcnt lgkmcnt(0)\n\t"
    "v_cmp_gt_u32 vcc, s61, v249\n\t"
    "s_cbranch_vccnz .Lsv2%=\n\t"
    "s_branch .Lrv2%=\n\t"
    ".Lsv3%=:\n\t"
    "s_sleep 1\n\t"
    "ds_read_b32 v250, %[fl4] offset:0\n\t"
    "s_waitcnt lgkmcnt(0)\n\t"
    "v_cmp_gt_u32 vcc, s62, v250\n\t"
    "s_cbranch_vccnz .Lsv3%=\n\t"
    "s_branch .Lrv3%=\n\t"
    ".Lsv4%=:\n\t"
    "s_sleep 1\n\t"
    "ds_read_b32 v249, %[fl4] offset:32\n\t"
    "s_waitcnt lgkmcnt(0)\n\t"
    "v_cmp_gt_u32 vcc, s61, v249\n\t"
    "s_cbranch_vccnz .Lsv4%=\n\t"
    "s_branch .Lrv4%=\n\t"
    ".Lsv5%=:\n\t"
    "s_sleep 1\n\t"
    "ds_read_b32 v250, %[fl4] offset:0\n\t"
    "s_waitcnt lgkmcnt(0)\n\t"
    "v_cmp_gt_u32 vcc, s62, v250\n\t"
    "s_cbranch_vccnz .Lsv5%=\n\t"
    "s_branch .Lrv5%=\n\t"
    ".Lendv%=:\n\t"
    :: [rf] "s"(rf), [wb] "s"(wb), [l16] "v"(lane * 16), [xa0] "v"(xa0), [wa] "v"(wa), [fl4] "v"(lb + LDS_F + (lane & 3) * 4), [own] "v"(lb + LDS_F + w2 * 4)
    : "memory", "vcc", "scc", "s60", "s61", "s62", "s63", "s64", "s65", "v12", "v13", "v14", "v15", "v16", "v17", "v18", "v19", "v20", "v21", "v22", "v23", "v24", "v25", "v26", "v27", "v28", "v29", "v30", "v31", "v32", "v33", "v34", "v35", "v36", "v37", "v38", "v39", "v40", "v41", "v42", "v43", "v44", "v45", "v46", "v47", "v48", "v49", "v50", "v51", "v52", "v53", "v54", "v55", "v56", "v57", "v58", "v59", "v60", "v61", "v62", "v63", "v64", "v65", "v66", "v67", "v68", "v69", "v70", "v71", "v72", "v73", "v74", "v75", "v76", "v77", "v78", "v79", "v80", "v81", "v82", "v83", "v84", "v85", "v86", "v87", "v88", "v89", "v90", "v91", "v92", "v93", "v94", "v95", "v96", "v97", "v98", "v99", "v100", "v101", "v102", "v103", "v104", "v105", "v106", "v107", "v108", "v109", "v110", "v111", "v112", "v113", "v114", "v115", "v116", "v117", "v118", "v119", "v120", "v121", "v122", "v123", "v124", "v125", "v126", "v127", "v128", "v129", "v130", "v131", "v132", "v133", "v134", "v135", "v136", "v137", "v138", "v139", "v140", "v141", "v142", "v143", "v144", "v145", "v146", "v147", "v148", "v149", "v150", "v151", "v152", "v153", "v154", "v155", "v156", "v157", "v158", "v159", "v160", "v161", "v162", "v163", "v164", "v165", "v166", "v167", "v168", "v169", "v170", "v171", "v172", "v173", "v174", "v175", "v176", "v177", "v178", "v179", "v180", "v181", "v182", "v183", "v184", "v185", "v186", "v187", "v188", "v189", "v190", "v191", "v192", "v193", "v194", "v195", "v196", "v197", "v198", "v199", "v200", "v201", "v202", "v203", "v204", "v205", "v206", "v207", "v208", "v209", "v210", "v211", "v212", "v213", "v214", "v215", "v216", "v217", "v218", "v219", "v220", "v221", "v222", "v223", "v224", "v225", "v226", "v227", "v228", "v229", "v230", "v231", "v232", "v233", "v234", "v235", "v236", "v237", "v238", "v239", "v240", "v241", "v242", "v243", "v244", "v245", "v246", "v247", "v248", "v249", "v250", "v251", "v252", "v253", "v254", "v255");
  } else {
    asm volatile(
    "s_setprio 1\n\t"
    "v_mov_b32 v248, 1\n\t"
    "s_mov_b32 s61, 1\n\t"
    "s_add_u32 s65, %[wb], 0\n\t"
    "buffer_load_dwordx4 v[32:35], %[l16], %[rf], s65 offen\n\t"
    "buffer_load_dwordx4 v[36:39], %[l16], %[rf], s65 offen offset:1024\n\t"
    "buffer_load_dwordx4 v[40:43], %[l16], %[rf], s65 offen offset:2048\n\t"
    "buffer_load_dwordx4 v[44:47], %[l16], %[rf], s65 offen offset:3072\n\t"
    "s_add_u32 s65, %[wb], 4096\n\t"
    "buffer_load_dwordx4 v[48:51], %[l16], %[rf], s65 offen\n\t"
    "buffer_load_dwordx4 v[52:55], %[l16], %[rf], s65 offen offset:1024\n\t"
    "s_waitcnt lgkmcnt(0)\n\t"
    "s_barrier\n\t"
    "buffer_load_dwordx4 v[56:59], %[l16], %[rf], s65 offen offset:2048\n\t"
    "buffer_load_dwordx4 v[60:63], %[l16], %[rf], s65 offen offset:3072\n\t"
    "s_add_u32 s65, %[wb], 8192\n\t"
    "buffer_load_dwordx4 v[64:67], %[l16], %[rf], s65 offen\n\t"
    "buffer_load_dwordx4 v[68:71], %[l16], %[rf], s65 offen offset:1024\n\t"
    "buffer_load_dwordx4 v[72:75], %[l16], %[rf], s65 offen offset:2048\n\t"
    "buffer_load_dwordx4 v[76:79], %[l16], %[rf], s65 offen offset:3072\n\t"
    "s_add_u32 s65, %[wb], 12288\n\t"
    "buffer_load_dwordx4 v[80:83], %[l16], %[rf], s65 offen\n\t"
    "buffer_load_dwordx4 v[84:87], %[l16], %[rf], s65 offen offset:1024\n\t"
    "buffer_load_dwordx4 v[88:91], %[l16], %[rf], s65 offen offset:2048\n\t"
    "buffer_load_dwordx4 v[92:95], %[l16], %[rf], s65 offen offset:3072\n\t"
    "s_add_u32 s65, %[wb], 16384\n\t"
    "buffer_load_dwordx4 v[96:99], %[l16], %[rf], s65 offen\n\t"
    "buffer_load_dwordx4 v[100:103], %[l16], %[rf], s65 offen offset:1024\n\t"
    "buffer_load_dwordx4 v[104:107], %[l16], %[rf], s65 offen offset:2048\n\t"
    "buffer_load_dwordx4 v[108:111], %[l16], %[rf], s65 offen offset:3072\n\t"
    "s_add_u32 s65, %[wb], 20480\n\t"
    "buffer_load_dwordx4 v[112:115], %[l16], %[rf], s65 offen\n\t"
    "buffer_load_dwordx4 v[116:119], %[l16], %[rf], s65 offen offset:1024\n\t"
    "buffer_load_dwordx4 v[120:123], %[l16], %[rf], s65 offen offset:2048\n\t"
    "buffer_load_dwordx4 v[124:127], %[l16], %[rf], s65 offen offset:3072\n\t"
    "s_add_u32 s65, %[wb], 24576\n\t"
    "buffer_load_dwordx4 v[128:131], %[l16], %[rf], s65 offen\n\t"
    "buffer_load_dwordx4 v[132:135], %[l16], %[rf], s65 offen offset:1024\n\t"
    "buffer_load_dwordx4 v[136:139], %[l16], %[rf], s65 offen offset:2048\n\t"
    "buffer_load_dwordx4 v[140:143], %[l16], %[rf], s65 offen offset:3072\n\t"
    "s_add_u32 s65, %[wb], 28672\n\t"
    "buffer_load_dwordx4 v[144:147], %[l16], %[rf], s65 offen\n\t"
    "buffer_load_dwordx4 v[148:151], %[l16], %[rf], s65 offen offset:1024\n\t"
    "buffer_load_dwordx4 v[152:155], %[l16], %[rf], s65 offen offset:2048\n\t"
    "buffer_load_dwordx4 v[156:159], %[l16], %[rf], s65 offen offset:3072\n\t"
    "s_add_u32 s65, %[wb], 32768\n\t"
    "buffer_load_dwordx4 v[160:163], %[l16], %[rf], s65 offen\n\t"
    "buffer_load_dwordx4 v[164:167], %[l16], %[rf], s65 offen offset:1024\n\t"
    "buffer_load_dwordx4 v[168:171], %[l16], %[rf], s65 offen offset:2048\n\t"
    "buffer_load_dwordx4 v[172:175], %[l16], %[rf], s65 offen offset:3072\n\t"
    "v_mov_b32 v28, %[xa0]\n\t"
    "v_xor_b32 v29, 64, %[xa0]\n\t"
    "v_mov_b32 v30, %[wa]\n\t"
    "v_mov_b32 v248, 1\n\t"
    "s_mov_b32 s61, 1\n\t"
    "s_mov_b32 s62, 2\n\t"
    "s_mov_b32 s63, 0\n\t"
    "s_mov_b32 s64, 24576\n\t"
    "ds_read_b32 v249, %[fl4] offset:0\n\t"
    "s_waitcnt lgkmcnt(0)\n\t"
    "v_cmp_gt_u32 vcc, s61, v249\n\t"
    "s_cbranch_vccnz .Lst1%=\n\t"
    ".Lrt1%=:\n\t"
    "ds_read_b128 v[176:179], v28 offset:0\n\t"
    "ds_read_b128 v[180:183], v29 offset:0\n\t"
    "ds_read_b128 v[184:187], v28 offset:8192\n\t"
    "ds_read_b128 v[188:191], v29 offset:8192\n\t"
    "ds_read_b128 v[192:195], v28 offset:16384\n\t"
    "ds_read_b128 v[196:199], v29 offset:16384\n\t"
    "s_waitcnt lgkmcnt(0)\n\t"
    "s_waitcnt vmcnt(35)\n\t"
    "v_mfma_f32_16x16x32_bf16 v[12:15], v[32:35], v[176:179], 0\n\t"
    "s_waitcnt vmcnt(34)\n\t"
    "v_mfma_f32_16x16x32_bf16 v[12:15], v[36:39], v[180:183], v[12:15]\n\t"
    "s_waitcnt vmcnt(33)\n\t"
    "v_mfma_f32_16x16x32_bf16 v[12:15], v[40:43], v[184:187], v[12:15]\n\t"
    "s_waitcnt vmcnt(32)\n\t"
    "v_mfma_f32_16x16x32_bf16 v[12:15], v[44:47], v[188:191], v[12:15]\n\t"
    "s_waitcnt vmcnt(31)\n\t"
    "v_mfma_f32_16x16x32_bf16 v[12:15], v[48:51], v[192:195], v[12:15]\n\t"
    "ds_read_b32 v249, %[fz] offset:16\n\t"
    "s_waitcnt vmcnt(30)\n\t"
    "v_mfma_f32_16x16x32_bf16 v[12:15], v[52:55], v[196:199], v[12:15]\n\t"
    "s_waitcnt vmcnt(29)\n\t"
    "v_mfma_f32_16x16x32_bf16 v[16:19], v[56:59], v[176:179], 0\n\t"
    "ds_read_b128 v[200:203], v28 offset:2048\n\t"
    "s_waitcnt vmcnt(28)\n\t"
    "v_mfma_f32_16x16x32_bf16 v[16:19], v[60:63], v[180:183], v[16:19]\n\t"
    "v_add_u32 v248, 1, v248\n\t"
    "s_waitcnt vmcnt(27)\n\t"
    "v_mfma_f32_16x16x32_bf16 v[16:19], v[64:67], v[184:187], v[16:19]\n\t"
    "s_waitcnt vmcnt(26)\n\t"
    "v_mfma_f32_16x16x32_bf16 v[16:19], v[68:71], v[188:191], v[16:19]\n\t"
    "s_waitcnt vmcnt(25)\n\t"
    "v_mfma_f32_16x16x32_bf16 v[16:19], v[72:75], v[192:195], v[16:19]\n\t"
    "s_waitcnt vmcnt(24)\n\t"
    "v_mfma_f32_16x16x32_bf16 v[16:19], v[76:79], v[196:199], v[16:19]\n\t"
    "ds_read_b128 v[204:207], v29 offset:2048\n\t"
    "s_waitcnt lgkmcnt(2)\n\t"
    "v_cmp_gt_u32 vcc, s62, v249\n\t"
    "s_cbranch_vccnz .Lst2%=\n\t"
    ".Lrt2%=:\n\t"
    "s_waitcnt vmcnt(23)\n\t"
    "v_mfma_f32_16x16x32_bf16 v[20:23], v[80:83], v[176:179], 0\n\t"
    "ds_read_b128 v[208:211], v28 offset:10240\n\t"
    "s_waitcnt vmcnt(22)\n\t"
    "v_mfma_f32_16x16x32_bf16 v[20:23], v[84:87], v[180:183], v[20:23]\n\t"
    "v_cvt_pk_bf16_f32 v224, v12, v13\n\t"
    "s_waitcnt vmcnt(21)\n\t"
    "v_mfma_f32_16x16x32_bf16 v[20:23], v[88:91], v[184:187], v[20:23]\n\t"
    "v_cvt_pk_bf16_f32 v225, v14, v15\n\t"
    "s_waitcnt vmcnt(20)\n\t"
    "v_mfma_f32_16x16x32_bf16 v[20:23], v[92:95], v[188:191], v[20:23]\n\t"
    "ds_write_b64 v30, v[224:225] offset:0\n\t"
    "s_waitcnt vmcnt(19)\n\t"
    "v_mfma_f32_16x16x32_bf16 v[20:23], v[96:99], v[192:195], v[20:23]\n\t"
    "s_waitcnt vmcnt(18)\n\t"
    "v_mfma_f32_16x16x32_bf16 v[20:23], v[100:103], v[196:199], v[20:23]\n\t"
    "ds_read_b128 v[212:215], v29 offset:10240\n\t"
    "s_waitcnt vmcnt(17)\n\t"
    "v_mfma_f32_16x16x32_bf16 v[12:15], v[104:107], v[176:179], 0\n\t"
    "ds_read_b128 v[216:219], v28 offset:18432\n\t"
    "s_waitcnt vmcnt(16)\n\t"
    "v_mfma_f32_16x16x32_bf16 v[12:15], v[108:111], v[180:183], v[12:15]\n\t"
    "v_cvt_pk_bf16_f32 v226, v16, v17\n\t"
    "s_waitcnt vmcnt(15)\n\t"
    "v_mfma_f32_16x16x32_bf16 v[12:15], v[112:115], v[184:187], v[12:15]\n\t"
    "v_cvt_pk_bf16_f32 v227, v18, v19\n\t"
    "s_waitcnt vmcnt(14)\n\t"
    "v_mfma_f32_16x16x32_bf16 v[12:15], v[116:119], v[188:191], v[12:15]\n\t"
    "ds_write_b64 v30, v[226:227] offset:8\n\t"
    "s_waitcnt vmcnt(13)\n\t"
    "v_mfma_f32_16x16x32_bf16 v[12:15], v[120:123], v[192:195], v[12:15]\n\t"
    "s_waitcnt vmcnt(12)\n\t"
    "v_mfma_f32_16x16x32_bf16 v[12:15], v[124:127], v[196:199], v[12:15]\n\t"
    "ds_read_b128 v[220:223], v29 offset:18432\n\t"
    "s_waitcnt vmcnt(11)\n\t"
    "v_mfma_f32_16x16x32_bf16 v[16:19], v[128:131], v[176:179], 0\n\t"
    "s_waitcnt vmcnt(10)\n\t"
    "v_mfma_f32_16x16x32_bf16 v[16:19], v[132:135], v[180:183], v[16:19]\n\t"
    "v_cvt_pk_bf16_f32 v224, v20, v21\n\t"
    "s_waitcnt vmcnt(9)\n\t"
    "v_mfma_f32_16x16x32_bf16 v[16:19], v[136:139], v[184:187], v[16:19]\n\t"
    "v_cvt_pk_bf16_f32 v225, v22, v23\n\t"
    "s_waitcnt vmcnt(8)\n\t"
    "v_mfma_f32_16x16x32_bf16 v[16:19], v[140:143], v[188:191], v[16:19]\n\t"
    "ds_write_b64 v30, v[224:225] offset:1024\n\t"
    "s_waitcnt vmcnt(7)\n\t"
    "v_mfma_f32_16x16x32_bf16 v[16:19], v[144:147], v[192:195], v[16:19]\n\t"
    "s_waitcnt vmcnt(6)\n\t"
    "v_mfma_f32_16x16x32_bf16 v[16:19], v[148:151], v[196:199], v[16:19]\n\t"
    "s_waitcnt vmcnt(5)\n\t"
    "v_mfma_f32_16x16x32_bf16 v[20:23], v[152:155], v[176:179], 0\n\t"
    "s_waitcnt vmcnt(4)\n\t"
    "v_mfma_f32_16x16x32_bf16 v[20:23], v[156:159], v[180:183], v[20:23]\n\t"
    "v_cvt_pk_bf16_f32 v226, v12, v13\n\t"
    "s_waitcnt vmcnt(3)\n\t"
    "v_mfma_f32_16x16x32_bf16 v[20:23], v[160:163], v[184:187], v[20:23]\n\t"
    "v_cvt_pk_bf16_f32 v227, v14, v15\n\t"
    "s_waitcnt vmcnt(2)\n\t"
    "v_mfma_f32_16x16x32_bf16 v[20:23], v[164:167], v[188:191], v[20:23]\n\t"
    "ds_write_b64 v30, v[226:227] offset:1032\n\t"
    "s_waitcnt vmcnt(1)\n\t"
    "v_mfma_f32_16x16x32_bf16 v[20:23], v[168:171], v[192:195], v[20:23]\n\t"
    "s_waitcnt vmcnt(0)\n\t"
    "v_mfma_f32_16x16x32_bf16 v[20:23], v[172:175], v[196:199], v[20:23]\n\t"
    "s_waitcnt lgkmcnt(2)\n\t"
    "v_mfma_f32_16x16x32_bf16 v[12:15], v[32:35], v[200:203], 0\n\t"
    "v_mfma_f32_16x16x32_bf16 v[12:15], v[36:39], v[204:207], v[12:15]\n\t"
    "v_cvt_pk_bf16_f32 v224, v16, v17\n\t"
    "v_mfma_f32_16x16x32_bf16 v[12:15], v[40:43], v[208:211], v[12:15]\n\t"
    "v_cvt_pk_bf16_f32 v225, v18, v19\n\t"
    "v_mfma_f32_16x16x32_bf16 v[12:15], v[44:47], v[212:215], v[12:15]\n\t"
    "ds_write_b64 v30, v[224:225] offset:2048\n\t"
    "v_mfma_f32_16x16x32_bf16 v[12:15], v[48:51], v[216:219], v[12:15]\n\t"
    "ds_read_b32 v249, %[fz] offset:20\n\t"
    "v_mfma_f32_16x16x32_bf16 v[12:15], v[52:55], v[220:223], v[12:15]\n\t"
    "v_mfma_f32_16x16x32_bf16 v[16:19], v[56:59], v[200:203], 0\n\t"
    "ds_read_b128 v[176:179], v28 offset:4096\n\t"
    "v_mfma_f32_16x16x32_bf16 v[16:19], v[60:63], v[204:207], v[16:19]\n\t"
    "v_cvt_pk_bf16_f32 v226, v20, v21\n\t"
    "v_mfma_f32_16x16x32_bf16 v[16:19], v[64:67], v[208:211], v[16:19]\n\t"
    "v_cvt_pk_bf16_f32 v227, v22, v23\n\t"
    "v_mfma_f32_16x16x32_bf16 v[16:19], v[68:71], v[212:215], v[16:19]\n\t"
    "ds_write_b64 v30, v[226:227] offset:2056\n\t"
    "v_mfma_f32_16x16x32_bf16 v[16:19], v[72:75], v[216:219], v[16:19]\n\t"
    "ds_write_b32 %[own], v248 offset:64\n\t"
    "v_mfma_f32_16x16x32_bf16 v[16:19], v[76:79], v[220:223], v[16:19]\n\t"
    "ds_read_b128 v[180:183], v29 offset:4096\n\t"
    "s_waitcnt lgkmcnt(4)\n\t"
    "v_cmp_gt_u32 vcc, s62, v249\n\t"
    "s_cbranch_vccnz .Lst3%=\n\t"
    ".Lrt3%=:\n\t"
    "v_mfma_f32_16x16x32_bf16 v[20:23], v[80:83], v[200:203], 0\n\t"
    "ds_read_b128 v[184:187], v28 offset:12288\n\t"
    "v_mfma_f32_16x16x32_bf16 v[20:23], v[84:87], v[204:207], v[20:23]\n\t"
    "v_cvt_pk_bf16_f32 v224, v12, v13\n\t"
    "v_mfma_f32_16x16x32_bf16 v[20:23], v[88:91], v[208:211], v[20:23]\n\t"
    "v_cvt_pk_bf16_f32 v225, v14, v15\n\t"
    "v_mfma_f32_16x16x32_bf16 v[20:23], v[92:95], v[212:215], v[20:23]\n\t"
    "ds_write_b64 v30, v[224:225] offset:6144\n\t"
    "v_mfma_f32_16x16x32_bf16 v[20:23], v[96:99], v[216:219], v[20:23]\n\t"
    "v_mfma_f32_16x16x32_bf16 v[20:23], v[100:103], v[220:223], v[20:23]\n\t"
    "ds_read_b128 v[188:191], v29 offset:12288\n\t"
    "v_mfma_f32_16x16x32_bf16 v[12:15], v[104:107], v[200:203], 0\n\t"
    "ds_read_b128 v[192:195], v28 offset:20480\n\t"
    "v_mfma_f32_16x16x32_bf16 v[12:15], v[108:111], v[204:207], v[12:15]\n\t"
    "v_cvt_pk_bf16_f32 v226, v16, v17\n\t"
    "v_mfma_f32_16x16x32_bf16 v[12:15], v[112:115], v[208:211], v[12:15]\n\t"
    "v_cvt_pk_bf16_f32 v227, v18, v19\n\t"
    "v_mfma_f32_16x16x32_bf16 v[12:15], v[116:119], v[212:215], v[12:15]\n\t"
    "ds_write_b64 v30, v[226:227] offset:6152\n\t"
    "v_mfma_f32_16x16x32_bf16 v[12:15], v[120:123], v[216:219], v[12:15]\n\t"
    "v_mfma_f32_16x16x32_bf16 v[12:15], v[124:127], v[220:223], v[12:15]\n\t"
    "ds_read_b128 v[196:199], v29 offset:20480\n\t"
    "v_mfma_f32_16x16x32_bf16 v[16:19], v[128:131], v[200:203], 0\n\t"
    "v_mfma_f32_16x16x32_bf16 v[16:19], v[132:135], v[204:207], v[16:19]\n\t"
    "v_cvt_pk_bf16_f32 v224, v20, v21\n\t"
    "v_mfma_f32_16x16x32_bf16 v[16:19], v[136:139], v[208:211], v[16:19]\n\t"
    "v_cvt_pk_bf16_f32 v225, v22, v23\n\t"
    "v_mfma_f32_16x16x32_bf16 v[16:19], v[140:143], v[212:215], v[16:19]\n\t"
    "ds_write_b64 v30, v[224:225] offset:7168\n\t"
    "v_mfma_f32_16x16x32_bf16 v[16:19], v[144:147], v[216:219], v[16:19]\n\t"
    "v_mfma_f32_16x16x32_bf16 v[16:19], v[148:151], v[220:223], v[16:19]\n\t"
    "v_mfma_f32_16x16x32_bf16 v[20:23], v[152:155], v[200:203], 0\n\t"
    "v_mfma_f32_16x16x32_bf16 v[20:23], v[156:159], v[204:207], v[20:23]\n\t"
    "v_cvt_pk_bf16_f32 v226, v12, v13\n\t"
    "v_mfma_f32_16x16x32_bf16 v[20:23], v[160:163], v[208:211], v[20:23]\n\t"
    "v_cvt_pk_bf16_f32 v227, v14, v15\n\t"
    "v_mfma_f32_16x16x32_bf16 v[20:23], v[164:167], v[212:215], v[20:23]\n\t"
    "ds_write_b64 v30, v[226:227] offset:7176\n\t"
    "v_mfma_f32_16x16x32_bf16 v[20:23], v[168:171], v[216:219], v[20:23]\n\t"
    "v_mfma_f32_16x16x32_bf16 v[20:23], v[172:175], v[220:223], v[20:23]\n\t"
    "s_waitcnt lgkmcnt(2)\n\t"
    "v_mfma_f32_16x16x32_bf16 v[12:15], v[32:35], v[176:179], 0\n\t"
    "v_mfma_f32_16x16x32_bf16 v[12:15], v[36:39], v[180:183], v[12:15]\n\t"
    "v_cvt_pk_bf16_f32 v224, v16, v17\n\t"
    "v_mfma_f32_16x16x32_bf16 v[12:15], v[40:43], v[184:187], v[12:15]\n\t"
    "v_cvt_pk_bf16_f32 v225, v18, v19\n\t"
    "v_mfma_f32_16x16x32_bf16 v[12:15], v[44:47], v[188:191], v[12:15]\n\t"
    "ds_write_b64 v30, v[224:225] offset:8192\n\t"
    "v_mfma_f32_16x16x32_bf16 v[12:15], v[48:51], v[192:195], v[12:15]\n\t"
    "ds_read_b32 v249, %[fz] offset:24\n\t"
    "v_mfma_f32_16x16x32_bf16 v[12:15], v[52:55], v[196:199], v[12:15]\n\t"
    "v_mfma_f32_16x16x32_bf16 v[16:19], v[56:59], v[176:179], 0\n\t"
    "ds_read_b128 v[200:203], v28 offset:6144\n\t"
    "v_mfma_f32_16x16x32_bf16 v[16:19], v[60:63], v[180:183], v[16:19]\n\t"
    "v_cvt_pk_bf16_f32 v226, v20, v21\n\t"
    "v_mfma_f32_16x16x32_bf16 v[16:19], v[64:67], v[184:187], v[16:19]\n\t"
    "v_cvt_pk_bf16_f32 v227, v22, v23\n\t"
    "v_mfma_f32_16x16x32_bf16 v[16:19], v[68:71], v[188:191], v[16:19]\n\t"
    "ds_write_b64 v30, v[226:227] offset:8200\n\t"
    "v_mfma_f32_16x16x32_bf16 v[16:19], v[72:75], v[192:195], v[16:19]\n\t"
    "ds_write_b32 %[own], v248 offset:72\n\t"
    "v_mfma_f32_16x16x32_bf16 v[16:19], v[76:79], v[196:199], v[16:19]\n\t"
    "ds_read_b128 v[204:207], v29 offset:6144\n\t"
    "s_waitcnt lgkmcnt(4)\n\t"
    "v_cmp_gt_u32 vcc, s62, v249\n\t"
    "s_cbranch_vccnz .Lst4%=\n\t"
    ".Lrt4%=:\n\t"
    "v_mfma_f32_16x16x32_bf16 v[20:23], v[80:83], v[176:179], 0\n\t"
    "ds_read_b128 v[208:211], v28 offset:14336\n\t"
    "v_mfma_f32_16x16x32_bf16 v[20:23], v[84:87], v[180:183], v[20:23]\n\t"
    "v_cvt_pk_bf16_f32 v224, v12, v13\n\t"
    "v_mfma_f32_16x16x32_bf16 v[20:23], v[88:91], v[184:187], v[20:23]\n\t"
    "v_cvt_pk_bf16_f32 v225, v14, v15\n\t"
    "v_mfma_f32_16x16x32_bf16 v[20:23], v[92:95], v[188:191], v[20:23]\n\t"
    "ds_write_b64 v30, v[224:225] offset:12288\n\t"
    "v_mfma_f32_16x16x32_bf16 v[20:23], v[96:99], v[192:195], v[20:23]\n\t"
    "v_mfma_f32_16x16x32_bf16 v[20:23], v[100:103], v[196:199], v[20:23]\n\t"
    "ds_read_b128 v[212:215], v29 offset:14336\n\t"
    "v_mfma_f32_16x16x32_bf16 v[12:15], v[104:107], v[176:179], 0\n\t"
    "ds_read_b128 v[216:219], v28 offset:22528\n\t"
    "v_mfma_f32_16x16x32_bf16 v[12:15], v[108:111], v[180:183], v[12:15]\n\t"
    "v_cvt_pk_bf16_f32 v226, v16, v17\n\t"
    "v_mfma_f32_16x16x32_bf16 v[12:15], v[112:115], v[184:187], v[12:15]\n\t"
    "v_cvt_pk_bf16_f32 v227, v18, v19\n\t"
    "v_mfma_f32_16x16x32_bf16 v[12:15], v[116:119], v[188:191], v[12:15]\n\t"
    "ds_write_b64 v30, v[226:227] offset:12296\n\t"
    "v_mfma_f32_16x16x32_bf16 v[12:15], v[120:123], v[192:195], v[12:15]\n\t"
    "v_mfma_f32_16x16x32_bf16 v[12:15], v[124:127], v[196:199], v[12:15]\n\t"
    "ds_read_b128 v[220:223], v29 offset:22528\n\t"
    "v_mfma_f32_16x16x32_bf16 v[16:19], v[128:131], v[176:179], 0\n\t"
    "ds_read_b32 v250, %[fl4] offset:0\n\t"
    "v_mfma_f32_16x16x32_bf16 v[16:19], v[132:135], v[180:183], v[16:19]\n\t"
    "v_cvt_pk_bf16_f32 v224, v20, v21\n\t"
    "v_mfma_f32_16x16x32_bf16 v[16:19], v[136:139], v[184:187], v[16:19]\n\t"
    "v_cvt_pk_bf16_f32 v225, v22, v23\n\t"
    "v_mfma_f32_16x16x32_bf16 v[16:19], v[140:143], v[188:191], v[16:19]\n\t"
    "ds_write_b64 v30, v[224:225] offset:13312\n\t"
    "v_mfma_f32_16x16x32_bf16 v[16:19], v[144:147], v[192:195], v[16:19]\n\t"
    "v_mfma_f32_16x16x32_bf16 v[16:19], v[148:151], v[196:199], v[16:19]\n\t"
    "v_mfma_f32_16x16x32_bf16 v[20:23], v[152:155], v[176:179], 0\n\t"
    "s_cmp_eq_u32 s63, 2\n\t"
    "s_cselect_b32 s60, 4294918144, s64\n\t"
    "v_mfma_f32_16x16x32_bf16 v[20:23], v[156:159], v[180:183], v[20:23]\n\t"
    "v_cvt_pk_bf16_f32 v226, v12, v13\n\t"
    "v_mfma_f32_16x16x32_bf16 v[20:23], v[160:163], v[184:187], v[20:23]\n\t"
    "v_cvt_pk_bf16_f32 v227, v14, v15\n\t"
    "v_mfma_f32_16x16x32_bf16 v[20:23], v[164:167], v[188:191], v[20:23]\n\t"
    "ds_write_b64 v30, v[226:227] offset:13320\n\t"
    "v_mfma_f32_16x16x32_bf16 v[20:23], v[168:171], v[192:195], v[20:23]\n\t"
    "s_cselect_b32 s63, -1, s63\n\t"
    "v_mfma_f32_16x16x32_bf16 v[20:23], v[172:175], v[196:199], v[20:23]\n\t"
    "s_waitcnt lgkmcnt(2)\n\t"
    "v_cmp_gt_u32 vcc, s62, v250\n\t"
    "s_cbranch_vccnz .Lst5%=\n\t"
    ".Lrt5%=:\n\t"
    "v_mfma_f32_16x16x32_bf16 v[12:15], v[32:35], v[200:203], 0\n\t"
    "v_add_u32 v28, s60, v28\n\t"
    "v_mfma_f32_16x16x32_bf16 v[12:15], v[36:39], v[204:207], v[12:15]\n\t"
    "v_cvt_pk_bf16_f32 v224, v16, v17\n\t"
    "v_mfma_f32_16x16x32_bf16 v[12:15], v[40:43], v[208:211], v[12:15]\n\t"
    "v_cvt_pk_bf16_f32 v225, v18, v19\n\t"
    "v_mfma_f32_16x16x32_bf16 v[12:15], v[44:47], v[212:215], v[12:15]\n\t"
    "ds_write_b64 v30, v[224:225] offset:14336\n\t"
    "v_mfma_f32_16x16x32_bf16 v[12:15], v[48:51], v[216:219], v[12:15]\n\t"
    "v_add_u32 v29, s60, v29\n\t"
    "ds_read_b32 v249, %[fz] offset:28\n\t"
    "v_mfma_f32_16x16x32_bf16 v[12:15], v[52:55], v[220:223], v[12:15]\n\t"
    "s_add_u32 s63, s63, 1\n\t"
    "v_mfma_f32_16x16x32_bf16 v[16:19], v[56:59], v[200:203], 0\n\t"
    "ds_read_b128 v[176:179], v28 offset:0\n\t"
    "v_mfma_f32_16x16x32_bf16 v[16:19], v[60:63], v[204:207], v[16:19]\n\t"
    "v_cvt_pk_bf16_f32 v226, v20, v21\n\t"
    "v_mfma_f32_16x16x32_bf16 v[16:19], v[64:67], v[208:211], v[16:19]\n\t"
    "v_cvt_pk_bf16_f32 v227, v22, v23\n\t"
    "v_mfma_f32_16x16x32_bf16 v[16:19], v[68:71], v[212:215], v[16:19]\n\t"
    "ds_write_b64 v30, v[226:227] offset:14344\n\t"
    "v_mfma_f32_16x16x32_bf16 v[16:19], v[72:75], v[216:219], v[16:19]\n\t"
    "ds_write_b32 %[own], v248 offset:80\n\t"
    "v_mfma_f32_16x16x32_bf16 v[16:19], v[76:79], v[220:223], v[16:19]\n\t"
    "ds_read_b128 v[180:183], v29 offset:0\n\t"
    "s_waitcnt lgkmcnt(4)\n\t"
    "v_cmp_gt_u32 vcc, s62, v249\n\t"
    "s_cbranch_vccnz .Lst6%=\n\t"
    ".Lrt6%=:\n\t"
    "v_mfma_f32_16x16x32_bf16 v[20:23], v[80:83], v[200:203], 0\n\t"
    "ds_read_b128 v[184:187], v28 offset:8192\n\t"
    "v_mfma_f32_16x16x32_bf16 v[20:23], v[84:87], v[204:207], v[20:23]\n\t"
    "v_cvt_pk_bf16_f32 v224, v12, v13\n\t"
    "v_mfma_f32_16x16x32_bf16 v[20:23], v[88:91], v[208:211], v[20:23]\n\t"
    "v_cvt_pk_bf16_f32 v225, v14, v15\n\t"
    "v_mfma_f32_16x16x32_bf16 v[20:23], v[92:95], v[212:215], v[20:23]\n\t"
    "ds_write_b64 v30, v[224:225] offset:18432\n\t"
    "v_mfma_f32_16x16x32_bf16 v[20:23], v[96:99], v[216:219], v[20:23]\n\t"
    "v_mfma_f32_16x16x32_bf16 v[20:23], v[100:103], v[220:223], v[20:23]\n\t"
    "ds_read_b128 v[188:191], v29 offset:8192\n\t"
    "v_mfma_f32_16x16x32_bf16 v[12:15], v[104:107], v[200:203], 0\n\t"
    "ds_read_b128 v[192:195], v28 offset:16384\n\t"
    "v_mfma_f32_16x16x32_bf16 v[12:15], v[108:111], v[204:207], v[12:15]\n\t"
    "v_cvt_pk_bf16_f32 v226, v16, v17\n\t"
    "v_mfma_f32_16x16x32_bf16 v[12:15], v[112:115], v[208:211], v[12:15]\n\t"
    "v_cvt_pk_bf16_f32 v227, v18, v19\n\t"
    "v_mfma_f32_16x16x32_bf16 v[12:15], v[116:119], v[212:215], v[12:15]\n\t"
    "ds_write_b64 v30, v[226:227] offset:18440\n\t"
    "v_mfma_f32_16x16x32_bf16 v[12:15], v[120:123], v[216:219], v[12:15]\n\t"
    "v_mfma_f32_16x16x32_bf16 v[12:15], v[124:127], v[220:223], v[12:15]\n\t"
    "ds_read_b128 v[196:199], v29 offset:16384\n\t"
    "v_mfma_f32_16x16x32_bf16 v[16:19], v[128:131], v[200:203], 0\n\t"
    "v_mfma_f32_16x16x32_bf16 v[16:19], v[132:135], v[204:207], v[16:19]\n\t"
    "v_cvt_pk_bf16_f32 v224, v20, v21\n\t"
    "v_mfma_f32_16x16x32_bf16 v[16:19], v[136:139], v[208:211], v[16:19]\n\t"
    "v_cvt_pk_bf16_f32 v225, v22, v23\n\t"
    "v_mfma_f32_16x16x32_bf16 v[16:19], v[140:143], v[212:215], v[16:19]\n\t"
    "ds_write_b64 v30, v[224:225] offset:19456\n\t"
    "v_mfma_f32_16x16x32_bf16 v[16:19], v[144:147], v[216:219], v[16:19]\n\t"
    "v_mfma_f32_16x16x32_bf16 v[16:19], v[148:151], v[220:223], v[16:19]\n\t"
    "v_mfma_f32_16x16x32_bf16 v[20:23], v[152:155], v[200:203], 0\n\t"
    "s_add_u32 s61, s61, 1\n\t"
    "s_add_u32 s62, s62, 1\n\t"
    "v_mfma_f32_16x16x32_bf16 v[20:23], v[156:159], v[204:207], v[20:23]\n\t"
    "v_cvt_pk_bf16_f32 v226, v12, v13\n\t"
    "v_mfma_f32_16x16x32_bf16 v[20:23], v[160:163], v[208:211], v[20:23]\n\t"
    "v_cvt_pk_bf16_f32 v227, v14, v15\n\t"
    "v_mfma_f32_16x16x32_bf16 v[20:23], v[164:167], v[212:215], v[20:23]\n\t"
    "ds_write_b64 v30, v[226:227] offset:19464\n\t"
    "v_mfma_f32_16x16x32_bf16 v[20:23], v[168:171], v[216:219], v[20:23]\n\t"
    "v_mfma_f32_16x16x32_bf16 v[20:23], v[172:175], v[220:223], v[20:23]\n\t"
    "s_cmp_le_u32 s61, 9\n\t"
    ".Lloopt%=:\n\t"
    "s_waitcnt lgkmcnt(2)\n\t"
    "v_mfma_f32_16x16x32_bf16 v[12:15], v[32:35], v[176:179], 0\n\t"
    "v_mfma_f32_16x16x32_bf16 v[12:15], v[36:39], v[180:183], v[12:15]\n\t"
    "v_cvt_pk_bf16_f32 v224, v16, v17\n\t"
    "v_mfma_f32_16x16x32_bf16 v[12:15], v[40:43], v[184:187], v[12:15]\n\t"
    "v_cvt_pk_bf16_f32 v225, v18, v19\n\t"
    "v_mfma_f32_16x16x32_bf16 v[12:15], v[44:47], v[188:191], v[12:15]\n\t"
    "ds_write_b64 v30, v[224:225] offset:20480\n\t"
    "v_mfma_f32_16x16x32_bf16 v[12:15], v[48:51], v[192:195], v[12:15]\n\t"
    "ds_read_b32 v249, %[fz] offset:16\n\t"
    "v_mfma_f32_16x16x32_bf16 v[12:15], v[52:55], v[196:199], v[12:15]\n\t"
    "v_mfma_f32_16x16x32_bf16 v[16:19], v[56:59], v[176:179], 0\n\t"
    "ds_read_b128 v[200:203], v28 offset:2048\n\t"
    "v_mfma_f32_16x16x32_bf16 v[16:19], v[60:63], v[180:183], v[16:19]\n\t"
    "v_cvt_pk_bf16_f32 v226, v20, v21\n\t"
    "v_mfma_f32_16x16x32_bf16 v[16:19], v[64:67], v[184:187], v[16:19]\n\t"
    "v_cvt_pk_bf16_f32 v227, v22, v23\n\t"
    "v_mfma_f32_16x16x32_bf16 v[16:19], v[68:71], v[188:191], v[16:19]\n\t"
    "ds_write_b64 v30, v[226:227] offset:20488\n\t"
    "v_mfma_f32_16x16x32_bf16 v[16:19], v[72:75], v[192:195], v[16:19]\n\t"
    "ds_write_b32 %[own], v248 offset:88\n\t"
    "v_mfma_f32_16x16x32_bf16 v[16:19], v[76:79], v[196:199], v[16:19]\n\t"
    "ds_read_b128 v[204:207], v29 offset:2048\n\t"
    "v_add_u32 v248, 1, v248\n\t"
    "s_waitcnt lgkmcnt(4)\n\t"
    "v_cmp_gt_u32 vcc, s62, v249\n\t"
    "s_cbranch_vccnz .Lst7%=\n\t"
    ".Lrt7%=:\n\t"
    "v_mfma_f32_16x16x32_bf16 v[20:23], v[80:83], v[176:179], 0\n\t"
    "ds_read_b128 v[208:211], v28 offset:10240\n\t"
    "v_mfma_f32_16x16x32_bf16 v[20:23], v[84:87], v[180:183], v[20:23]\n\t"
    "v_cvt_pk_bf16_f32 v224, v12, v13\n\t"
    "v_mfma_f32_16x16x32_bf16 v[20:23], v[88:91], v[184:187], v[20:23]\n\t"
    "v_cvt_pk_bf16_f32 v225, v14, v15\n\t"
    "v_mfma_f32_16x16x32_bf16 v[20:23], v[92:95], v[188:191], v[20:23]\n\t"
    "ds_write_b64 v30, v[224:225] offset:0\n\t"
    "v_mfma_f32_16x16x32_bf16 v[20:23], v[96:99], v[192:195], v[20:23]\n\t"
    "v_mfma_f32_16x16x32_bf16 v[20:23], v[100:103], v[196:199], v[20:23]\n\t"
    "ds_read_b128 v[212:215], v29 offset:10240\n\t"
    "v_mfma_f32_16x16x32_bf16 v[12:15], v[104:107], v[176:179], 0\n\t"
    "ds_read_b128 v[216:219], v28 offset:18432\n\t"
    "v_mfma_f32_16x16x32_bf16 v[12:15], v[108:111], v[180:183], v[12:15]\n\t"
    "v_cvt_pk_bf16_f32 v226, v16, v17\n\t"
    "v_mfma_f32_16x16x32_bf16 v[12:15], v[112:115], v[184:187], v[12:15]\n\t"
    "v_cvt_pk_bf16_f32 v227, v18, v19\n\t"
    "v_mfma_f32_16x16x32_bf16 v[12:15], v[116:119], v[188:191], v[12:15]\n\t"
    "ds_write_b64 v30, v[226:227] offset:8\n\t"
    "v_mfma_f32_16x16x32_bf16 v[12:15], v[120:123], v[192:195], v[12:15]\n\t"
    "v_mfma_f32_16x16x32_bf16 v[12:15], v[124:127], v[196:199], v[12:15]\n\t"
    "ds_read_b128 v[220:223], v29 offset:18432\n\t"
    "v_mfma_f32_16x16x32_bf16 v[16:19], v[128:131], v[176:179], 0\n\t"
    "v_mfma_f32_16x16x32_bf16 v[16:19], v[132:135], v[180:183], v[16:19]\n\t"
    "v_cvt_pk_bf16_f32 v224, v20, v21\n\t"
    "v_mfma_f32_16x16x32_bf16 v[16:19], v[136:139], v[184:187], v[16:19]\n\t"
    "v_cvt_pk_bf16_f32 v225, v22, v23\n\t"
    "v_mfma_f32_16x16x32_bf16 v[16:19], v[140:143], v[188:191], v[16:19]\n\t"
    "ds_write_b64 v30, v[224:225] offset:1024\n\t"
    "v_mfma_f32_16x16x32_bf16 v[16:19], v[144:147], v[192:195], v[16:19]\n\t"
    "v_mfma_f32_16x16x32_bf16 v[16:19], v[148:151], v[196:199], v[16:19]\n\t"
    "v_mfma_f32_16x16x32_bf16 v[20:23], v[152:155], v[176:179], 0\n\t"
    "v_mfma_f32_16x16x32_bf16 v[20:23], v[156:159], v[180:183], v[20:23]\n\t"
    "v_cvt_pk_bf16_f32 v226, v12, v13\n\t"
    "v_mfma_f32_16x16x32_bf16 v[20:23], v[160:163], v[184:187], v[20:23]\n\t"
    "v_cvt_pk_bf16_f32 v227, v14, v15\n\t"
    "v_mfma_f32_16x16x32_bf16 v[20:23], v[164:167], v[188:191], v[20:23]\n\t"
    "ds_write_b64 v30, v[226:227] offset:1032\n\t"
    "v_mfma_f32_16x16x32_bf16 v[20:23], v[168:171], v[192:195], v[20:23]\n\t"
    "v_mfma_f32_16x16x32_bf16 v[20:23], v[172:175], v[196:199], v[20:23]\n\t"
    "s_waitcnt lgkmcnt(2)\n\t"
    "v_mfma_f32_16x16x32_bf16 v[12:15], v[32:35], v[200:203], 0\n\t"
    "v_mfma_f32_16x16x32_bf16 v[12:15], v[36:39], v[204:207], v[12:15]\n\t"
    "v_cvt_pk_bf16_f32 v224, v16, v17\n\t"
    "v_mfma_f32_16x16x32_bf16 v[12:15], v[40:43], v[208:211], v[12:15]\n\t"
    "v_cvt_pk_bf16_f32 v225, v18, v19\n\t"
    "v_mfma_f32_16x16x32_bf16 v[12:15], v[44:47], v[212:215], v[12:15]\n\t"
    "ds_write_b64 v30, v[224:225] offset:2048\n\t"
    "v_mfma_f32_16x16x32_bf16 v[12:15], v[48:51], v[216:219], v[12:15]\n\t"
    "ds_read_b32 v249, %[fz] offset:20\n\t"
    "v_mfma_f32_16x16x32_bf16 v[12:15], v[52:55], v[220:223], v[12:15]\n\t"
    "v_mfma_f32_16x16x32_bf16 v[16:19], v[56:59], v[200:203], 0\n\t"
    "ds_read_b128 v[176:179], v28 offset:4096\n\t"
    "v_mfma_f32_16x16x32_bf16 v[16:19], v[60:63], v[204:207], v[16:19]\n\t"
    "v_cvt_pk_bf16_f32 v226, v20, v21\n\t"
    "v_mfma_f32_16x16x32_bf16 v[16:19], v[64:67], v[208:211], v[16:19]\n\t"
    "v_cvt_pk_bf16_f32 v227, v22, v23\n\t"
    "v_mfma_f32_16x16x32_bf16 v[16:19], v[68:71], v[212:215], v[16:19]\n\t"
    "ds_write_b64 v30, v[226:227] offset:2056\n\t"
    "v_mfma_f32_16x16x32_bf16 v[16:19], v[72:75], v[216:219], v[16:19]\n\t"
    "ds_write_b32 %[own], v248 offset:64\n\t"
    "v_mfma_f32_16x16x32_bf16 v[16:19], v[76:79], v[220:223], v[16:19]\n\t"
    "ds_read_b128 v[180:183], v29 offset:4096\n\t"
    "s_waitcnt lgkmcnt(4)\n\t"
    "v_cmp_gt_u32 vcc, s62, v249\n\t"
    "s_cbranch_vccnz .Lst8%=\n\t"
    ".Lrt8%=:\n\t"
    "v_mfma_f32_16x16x32_bf16 v[20:23], v[80:83], v[200:203], 0\n\t"
    "ds_read_b128 v[184:187], v28 offset:12288\n\t"
    "v_mfma_f32_16x16x32_bf16 v[20:23], v[84:87], v[204:207], v[20:23]\n\t"
    "v_cvt_pk_bf16_f32 v224, v12, v13\n\t"
    "v_mfma_f32_16x16x32_bf16 v[20:23], v[88:91], v[208:211], v[20:23]\n\t"
    "v_cvt_pk_bf16_f32 v225, v14, v15\n\t"
    "v_mfma_f32_16x16x32_bf16 v[20:23], v[92:95], v[212:215], v[20:23]\n\t"
    "ds_write_b64 v30, v[224:225] offset:6144\n\t"
    "v_mfma_f32_16x16x32_bf16 v[20:23], v[96:99], v[216:219], v[20:23]\n\t"
    "v_mfma_f32_16x16x32_bf16 v[20:23], v[100:103], v[220:223], v[20:23]\n\t"
    "ds_read_b128 v[188:191], v29 offset:12288\n\t"
    "v_mfma_f32_16x16x32_bf16 v[12:15], v[104:107], v[200:203], 0\n\t"
    "ds_read_b128 v[192:195], v28 offset:20480\n\t"
    "v_mfma_f32_16x16x32_bf16 v[12:15], v[108:111], v[204:207], v[12:15]\n\t"
    "v_cvt_pk_bf16_f32 v226, v16, v17\n\t"
    "v_mfma_f32_16x16x32_bf16 v[12:15], v[112:115], v[208:211], v[12:15]\n\t"
    "v_cvt_pk_bf16_f32 v227, v18, v19\n\t"
    "v_mfma_f32_16x16x32_bf16 v[12:15], v[116:119], v[212:215], v[12:15]\n\t"
    "ds_write_b64 v30, v[226:227] offset:6152\n\t"
    "v_mfma_f32_16x16x32_bf16 v[12:15], v[120:123], v[216:219], v[12:15]\n\t"
    "v_mfma_f32_16x16x32_bf16 v[12:15], v[124:127], v[220:223], v[12:15]\n\t"
    "ds_read_b128 v[196:199], v29 offset:20480\n\t"
    "v_mfma_f32_16x16x32_bf16 v[16:19], v[128:131], v[200:203], 0\n\t"
    "v_mfma_f32_16x16x32_bf16 v[16:19], v[132:135], v[204:207], v[16:19]\n\t"
    "v_cvt_pk_bf16_f32 v224, v20, v21\n\t"
    "v_mfma_f32_16x16x32_bf16 v[16:19], v[136:139], v[208:211], v[16:19]\n\t"
    "v_cvt_pk_bf16_f32 v225, v22, v23\n\t"
    "v_mfma_f32_16x16x32_bf16 v[16:19], v[140:143], v[212:215], v[16:19]\n\t"
    "ds_write_b64 v30, v[224:225] offset:7168\n\t"
    "v_mfma_f32_16x16x32_bf16 v[16:19], v[144:147], v[216:219], v[16:19]\n\t"
    "v_mfma_f32_16x16x32_bf16 v[16:19], v[148:151], v[220:223], v[16:19]\n\t"
    "v_mfma_f32_16x16x32_bf16 v[20:23], v[152:155], v[200:203], 0\n\t"
    "v_mfma_f32_16x16x32_bf16 v[20:23], v[156:159], v[204:207], v[20:23]\n\t"
    "v_cvt_pk_bf16_f32 v226, v12, v13\n\t"
    "v_mfma_f32_16x16x32_bf16 v[20:23], v[160:163], v[208:211], v[20:23]\n\t"
    "v_cvt_pk_bf16_f32 v227, v14, v15\n\t"
    "v_mfma_f32_16x16x32_bf16 v[20:23], v[164:167], v[212:215], v[20:23]\n\t"
    "ds_write_b64 v30, v[226:227] offset:7176\n\t"
    "v_mfma_f32_16x16x32_bf16 v[20:23], v[168:171], v[216:219], v[20:23]\n\t"
    "v_mfma_f32_16x16x32_bf16 v[20:23], v[172:175], v[220:223], v[20:23]\n\t"
    "s_waitcnt lgkmcnt(2)\n\t"
    "v_mfma_f32_16x16x32_bf16 v[12:15], v[32:35], v[176:179], 0\n\t"
    "v_mfma_f32_16x16x32_bf16 v[12:15], v[36:39], v[180:183], v[12:15]\n\t"
    "v_cvt_pk_bf16_f32 v224, v16, v17\n\t"
    "v_mfma_f32_16x16x32_bf16 v[12:15], v[40:43], v[184:187], v[12:15]\n\t"
    "v_cvt_pk_bf16_f32 v225, v18, v19\n\t"
    "v_mfma_f32_16x16x32_bf16 v[12:15], v[44:47], v[188:191], v[12:15]\n\t"
    "ds_write_b64 v30, v[224:225] offset:8192\n\t"
    "v_mfma_f32_16x16x32_bf16 v[12:15], v[48:51], v[192:195], v[12:15]\n\t"
    "ds_read_b32 v249, %[fz] offset:24\n\t"
    "v_mfma_f32_16x16x32_bf16 v[12:15], v[52:55], v[196:199], v[12:15]\n\t"
    "v_mfma_f32_16x16x32_bf16 v[16:19], v[56:59], v[176:179], 0\n\t"
    "ds_read_b128 v[200:203], v28 offset:6144\n\t"
    "v_mfma_f32_16x16x32_bf16 v[16:19], v[60:63], v[180:183], v[16:19]\n\t"
    "v_cvt_pk_bf16_f32 v226, v20, v21\n\t"
    "v_mfma_f32_16x16x32_bf16 v[16:19], v[64:67], v[184:187], v[16:19]\n\t"
    "v_cvt_pk_bf16_f32 v227, v22, v23\n\t"
    "v_mfma_f32_16x16x32_bf16 v[16:19], v[68:71], v[188:191], v[16:19]\n\t"
    "ds_write_b64 v30, v[226:227] offset:8200\n\t"
    "v_mfma_f32_16x16x32_bf16 v[16:19], v[72:75], v[192:195], v[16:19]\n\t"
    "ds_write_b32 %[own], v248 offset:72\n\t"
    "v_mfma_f32_16x16x32_bf16 v[16:19], v[76:79], v[196:199], v[16:19]\n\t"
    "ds_read_b128 v[204:207], v29 offset:6144\n\t"
    "s_waitcnt lgkmcnt(4)\n\t"
    "v_cmp_gt_u32 vcc, s62, v249\n\t"
    "s_cbranch_vccnz .Lst9%=\n\t"
    ".Lrt9%=:\n\t"
    "v_mfma_f32_16x16x32_bf16 v[20:23], v[80:83], v[176:179], 0\n\t"
    "ds_read_b128 v[208:211], v28 offset:14336\n\t"
    "v_mfma_f32_16x16x32_bf16 v[20:23], v[84:87], v[180:183], v[20:23]\n\t"
    "v_cvt_pk_bf16_f32 v224, v12, v13\n\t"
    "v_mfma_f32_16x16x32_bf16 v[20:23], v[88:91], v[184:187], v[20:23]\n\t"
    "v_cvt_pk_bf16_f32 v225, v14, v15\n\t"
    "v_mfma_f32_16x16x32_bf16 v[20:23], v[92:95], v[188:191], v[20:23]\n\t"
    "ds_write_b64 v30, v[224:225] offset:12288\n\t"
    "v_mfma_f32_16x16x32_bf16 v[20:23], v[96:99], v[192:195], v[20:23]\n\t"
    "v_mfma_f32_16x16x32_bf16 v[20:23], v[100:103], v[196:199], v[20:23]\n\t"
    "ds_read_b128 v[212:215], v29 offset:14336\n\t"
    "v_mfma_f32_16x16x32_bf16 v[12:15], v[104:107], v[176:179], 0\n\t"
    "ds_read_b128 v[216:219], v28 offset:22528\n\t"
    "v_mfma_f32_16x16x32_bf16 v[12:15], v[108:111], v[180:183], v[12:15]\n\t"
    "v_cvt_pk_bf16_f32 v226, v16, v17\n\t"
    "v_mfma_f32_16x16x32_bf16 v[12:15], v[112:115], v[184:187], v[12:15]\n\t"
    "v_cvt_pk_bf16_f32 v227, v18, v19\n\t"
    "v_mfma_f32_16x16x32_bf16 v[12:15], v[116:119], v[188:191], v[12:15]\n\t"
    "ds_write_b64 v30, v[226:227] offset:12296\n\t"
    "v_mfma_f32_16x16x32_bf16 v[12:15], v[120:123], v[192:195], v[12:15]\n\t"
    "v_mfma_f32_16x16x32_bf16 v[12:15], v[124:127], v[196:199], v[12:15]\n\t"
    "ds_read_b128 v[220:223], v29 offset:22528\n\t"
    "v_mfma_f32_16x16x32_bf16 v[16:19], v[128:131], v[176:179], 0\n\t"
    "ds_read_b32 v250, %[fl4] offset:0\n\t"
    "v_mfma_f32_16x16x32_bf16 v[16:19], v[132:135], v[180:183], v[16:19]\n\t"
    "v_cvt_pk_bf16_f32 v224, v20, v21\n\t"
    "v_mfma_f32_16x16x32_bf16 v[16:19], v[136:139], v[184:187], v[16:19]\n\t"
    "v_cvt_pk_bf16_f32 v225, v22, v23\n\t"
    "v_mfma_f32_16x16x32_bf16 v[16:19], v[140:143], v[188:191], v[16:19]\n\t"
    "ds_write_b64 v30, v[224:225] offset:13312\n\t"
    "v_mfma_f32_16x16x32_bf16 v[16:19], v[144:147], v[192:195], v[16:19]\n\t"
    "v_mfma_f32_16x16x32_bf16 v[16:19], v[148:151], v[196:199], v[16:19]\n\t"
    "v_mfma_f32_16x16x32_bf16 v[20:23], v[152:155], v[176:179], 0\n\t"
    "s_cmp_eq_u32 s63, 2\n\t"
    "s_cselect_b32 s60, 4294918144, s64\n\t"
    "v_mfma_f32_16x16x32_bf16 v[20:23], v[156:159], v[180:183], v[20:23]\n\t"
    "v_cvt_pk_bf16_f32 v226, v12, v13\n\t"
    "v_mfma_f32_16x16x32_bf16 v[20:23], v[160:163], v[184:187], v[20:23]\n\t"
    "v_cvt_pk_bf16_f32 v227, v14, v15\n\t"
    "v_mfma_f32_16x16x32_bf16 v[20:23], v[164:167], v[188:191], v[20:23]\n\t"
    "ds_write_b64 v30, v[226:227] offset:13320\n\t"
    "v_mfma_f32_16x16x32_bf16 v[20:23], v[168:171], v[192:195], v[20:23]\n\t"
    "s_cselect_b32 s63, -1, s63\n\t"
    "v_mfma_f32_16x16x32_bf16 v[20:23], v[172:175], v[196:199], v[20:23]\n\t"
    "s_waitcnt lgkmcnt(2)\n\t"
    "v_cmp_gt_u32 vcc, s62, v250\n\t"
    "s_cbranch_vccnz .Lst10%=\n\t"
    ".Lrt10%=:\n\t"
    "v_mfma_f32_16x16x32_bf16 v[12:15], v[32:35], v[200:203], 0\n\t"
    "v_add_u32 v28, s60, v28\n\t"
    "v_mfma_f32_16x16x32_bf16 v[12:15], v[36:39], v[204:207], v[12:15]\n\t"
    "v_cvt_pk_bf16_f32 v224, v16, v17\n\t"
    "v_mfma_f32_16x16x32_bf16 v[12:15], v[40:43], v[208:211], v[12:15]\n\t"
    "v_cvt_pk_bf16_f32 v225, v18, v19\n\t"
    "v_mfma_f32_16x16x32_bf16 v[12:15], v[44:47], v[212:215], v[12:15]\n\t"
    "ds_write_b64 v30, v[224:225] offset:14336\n\t"
    "v_mfma_f32_16x16x32_bf16 v[12:15], v[48:51], v[216:219], v[12:15]\n\t"
    "v_add_u32 v29, s60, v29\n\t"
    "ds_read_b32 v249, %[fz] offset:28\n\t"
    "v_mfma_f32_16x16x32_bf16 v[12:15], v[52:55], v[220:223], v[12:15]\n\t"
    "s_add_u32 s63, s63, 1\n\t"
    "v_mfma_f32_16x16x32_bf16 v[16:19], v[56:59], v[200:203], 0\n\t"
    "ds_read_b128 v[176:179], v28 offset:0\n\t"
    "v_mfma_f32_16x16x32_bf16 v[16:19], v[60:63], v[204:207], v[16:19]\n\t"
    "v_cvt_pk_bf16_f32 v226, v20, v21\n\t"
    "v_mfma_f32_16x16x32_bf16 v[16:19], v[64:67], v[208:211], v[16:19]\n\t"
    "v_cvt_pk_bf16_f32 v227, v22, v23\n\t"
    "v_mfma_f32_16x16x32_bf16 v[16:19], v[68:71], v[212:215], v[16:19]\n\t"
    "ds_write_b64 v30, v[226:227] offset:14344\n\t"
    "v_mfma_f32_16x16x32_bf16 v[16:19], v[72:75], v[216:219], v[16:19]\n\t"
    "ds_write_b32 %[own], v248 offset:80\n\t"
    "v_mfma_f32_16x16x32_bf16 v[16:19], v[76:79], v[220:223], v[16:19]\n\t"
    "ds_read_b128 v[180:183], v29 offset:0\n\t"
    "s_waitcnt lgkmcnt(4)\n\t"
    "v_cmp_gt_u32 vcc, s62, v249\n\t"
    "s_cbranch_vccnz .Lst11%=\n\t"
    ".Lrt11%=:\n\t"
    "v_mfma_f32_16x16x32_bf16 v[20:23], v[80:83], v[200:203], 0\n\t"
    "ds_read_b128 v[184:187], v28 offset:8192\n\t"
    "v_mfma_f32_16x16x32_bf16 v[20:23], v[84:87], v[204:207], v[20:23]\n\t"
    "v_cvt_pk_bf16_f32 v224, v12, v13\n\t"
    "v_mfma_f32_16x16x32_bf16 v[20:23], v[88:91], v[208:211], v[20:23]\n\t"
    "v_cvt_pk_bf16_f32 v225, v14, v15\n\t"
    "v_mfma_f32_16x16x32_bf16 v[20:23], v[92:95], v[212:215], v[20:23]\n\t"
    "ds_write_b64 v30, v[224:225] offset:18432\n\t"
    "v_mfma_f32_16x16x32_bf16 v[20:23], v[96:99], v[216:219], v[20:23]\n\t"
    "v_mfma_f32_16x16x32_bf16 v[20:23], v[100:103], v[220:223], v[20:23]\n\t"
    "ds_read_b128 v[188:191], v29 offset:8192\n\t"
    "v_mfma_f32_16x16x32_bf16 v[12:15], v[104:107], v[200:203], 0\n\t"
    "ds_read_b128 v[192:195], v28 offset:16384\n\t"
    "v_mfma_f32_16x16x32_bf16 v[12:15], v[108:111], v[204:207], v[12:15]\n\t"
    "v_cvt_pk_bf16_f32 v226, v16, v17\n\t"
    "v_mfma_f32_16x16x32_bf16 v[12:15], v[112:115], v[208:211], v[12:15]\n\t"
    "v_cvt_pk_bf16_f32 v227, v18, v19\n\t"
    "v_mfma_f32_16x16x32_bf16 v[12:15], v[116:119], v[212:215], v[12:15]\n\t"
    "ds_write_b64 v30, v[226:227] offset:18440\n\t"
    "v_mfma_f32_16x16x32_bf16 v[12:15], v[120:123], v[216:219], v[12:15]\n\t"
    "v_mfma_f32_16x16x32_bf16 v[12:15], v[124:127], v[220:223], v[12:15]\n\t"
    "ds_read_b128 v[196:199], v29 offset:16384\n\t"
    "v_mfma_f32_16x16x32_bf16 v[16:19], v[128:131], v[200:203], 0\n\t"
    "v_mfma_f32_16x16x32_bf16 v[16:19], v[132:135], v[204:207], v[16:19]\n\t"
    "v_cvt_pk_bf16_f32 v224, v20, v21\n\t"
    "v_mfma_f32_16x16x32_bf16 v[16:19], v[136:139], v[208:211], v[16:19]\n\t"
    "v_cvt_pk_bf16_f32 v225, v22, v23\n\t"
    "v_mfma_f32_16x16x32_bf16 v[16:19], v[140:143], v[212:215], v[16:19]\n\t"
    "ds_write_b64 v30, v[224:225] offset:19456\n\t"
    "v_mfma_f32_16x16x32_bf16 v[16:19], v[144:147], v[216:219], v[16:19]\n\t"
    "v_mfma_f32_16x16x32_bf16 v[16:19], v[148:151], v[220:223], v[16:19]\n\t"
    "v_mfma_f32_16x16x32_bf16 v[20:23], v[152:155], v[200:203], 0\n\t"
    "s_add_u32 s61, s61, 1\n\t"
    "s_add_u32 s62, s62, 1\n\t"
    "v_mfma_f32_16x16x32_bf16 v[20:23], v[156:159], v[204:207], v[20:23]\n\t"
    "v_cvt_pk_bf16_f32 v226, v12, v13\n\t"
    "v_mfma_f32_16x16x32_bf16 v[20:23], v[160:163], v[208:211], v[20:23]\n\t"
    "v_cvt_pk_bf16_f32 v227, v14, v15\n\t"
    "v_mfma_f32_16x16x32_bf16 v[20:23], v[164:167], v[212:215], v[20:23]\n\t"
    "ds_write_b64 v30, v[226:227] offset:19464\n\t"
    "v_mfma_f32_16x16x32_bf16 v[20:23], v[168:171], v[216:219], v[20:23]\n\t"
    "v_mfma_f32_16x16x32_bf16 v[20:23], v[172:175], v[220:223], v[20:23]\n\t"
    "s_cmp_le_u32 s61, 9\n\t"
    "s_cbranch_scc1 .Lloopt%=\n\t"
    "v_cvt_pk_bf16_f32 v224, v16, v17\n\t"
    "v_cvt_pk_bf16_f32 v225, v18, v19\n\t"
    "ds_write_b64 v30, v[224:225] offset:20480\n\t"
    "s_nop 2\n\t"
    "v_cvt_pk_bf16_f32 v226, v20, v21\n\t"
    "v_cvt_pk_bf16_f32 v227, v22, v23\n\t"
    "ds_write_b64 v30, v[226:227] offset:20488\n\t"
    "ds_write_b32 %[own], v248 offset:88\n\t"
    "v_add_u32 v248, 1, v248\n\t"
    "s_branch .Lendt%=\n\t"
    ".Lst1%=:\n\t"
    "s_sleep 1\n\t"
    "ds_read_b32 v249, %[fl4] offset:0\n\t"
    "s_waitcnt lgkmcnt(0)\n\t"
    "v_cmp_gt_u32 vcc, s61, v249\n\t"
    "s_cbranch_vccnz .Lst1%=\n\t"
    "s_branch .Lrt1%=\n\t"
    ".Lst2%=:\n\t"
    "s_sleep 1\n\t"
    "ds_read_b32 v249, %[fz] offset:16\n\t"
    "s_waitcnt lgkmcnt(0)\n\t"
    "v_cmp_gt_u32 vcc, s62, v249\n\t"
    "s_cbranch_vccnz .Lst2%=\n\t"
    "s_branch .Lrt2%=\n\t"
    ".Lst3%=:\n\t"
    "s_sleep 1\n\t"
    "ds_read_b32 v249, %[fz] offset:20\n\t"
    "s_waitcnt lgkmcnt(0)\n\t"
    "v_cmp_gt_u32 vcc, s62, v249\n\t"
    "s_cbranch_vccnz .Lst3%=\n\t"
    "s_branch .Lrt3%=\n\t"
    ".Lst4%=:\n\t"
    "s_sleep 1\n\t"
    "ds_read_b32 v249, %[fz] offset:24\n\t"
    "s_waitcnt lgkmcnt(0)\n\t"
    "v_cmp_gt_u32 vcc, s62, v249\n\t"
    "s_cbranch_vccnz .Lst4%=\n\t"
    "s_branch .Lrt4%=\n\t"
    ".Lst5%=:\n\t"
    "s_sleep 1\n\t"
    "ds_read_b32 v250, %[fl4] offset:0\n\t"
    "s_waitcnt lgkmcnt(0)\n\t"
    "v_cmp_gt_u32 vcc, s62, v250\n\t"
    "s_cbranch_vccnz .Lst5%=\n\t"
    "s_branch .Lrt5%=\n\t"
    ".Lst6%=:\n\t"
    "s_sleep 1\n\t"
    "ds_read_b32 v249, %[fz] offset:28\n\t"
    "s_waitcnt lgkmcnt(0)\n\t"
    "v_cmp_gt_u32 vcc, s62, v249\n\t"
    "s_cbranch_vccnz .Lst6%=\n\t"
    "s_branch .Lrt6%=\n\t"
    ".Lst7%=:\n\t"
    "s_sleep 1\n\t"
    "ds_read_b32 v249, %[fz] offset:16\n\t"
    "s_waitcnt lgkmcnt(0)\n\t"
    "v_cmp_gt_u32 vcc, s62, v249\n\t"
    "s_cbranch_vccnz .Lst7%=\n\t"
    "s_branch .Lrt7%=\n\t"
    ".Lst8%=:\n\t"
    "s_sleep 1\n\t"
    "ds_read_b32 v249, %[fz] offset:20\n\t"
    "s_waitcnt lgkmcnt(0)\n\t"
    "v_cmp_gt_u32 vcc, s62, v249\n\t"
    "s_cbranch_vccnz .Lst8%=\n\t"
    "s_branch .Lrt8%=\n\t"
    ".Lst9%=:\n\t"
    "s_sleep 1\n\t"
    "ds_read_b32 v249, %[fz] offset:24\n\t"
    "s_waitcnt lgkmcnt(0)\n\t"
    "v_cmp_gt_u32 vcc, s62, v249\n\t"
    "s_cbranch_vccnz .Lst9%=\n\t"
    "s_branch .Lrt9%=\n\t"
    ".Lst10%=:\n\t"
    "s_sleep 1\n\t"
    "ds_read_b32 v250, %[fl4] offset:0\n\t"
    "s_waitcnt lgkmcnt(0)\n\t"
    "v_cmp_gt_u32 vcc, s62, v250\n\t"
    "s_cbranch_vccnz .Lst10%=\n\t"
    "s_branch .Lrt10%=\n\t"
    ".Lst11%=:\n\t"
    "s_sleep 1\n\t"
    "ds_read_b32 v249, %[fz] offset:28\n\t"
    "s_waitcnt lgkmcnt(0)\n\t"
    "v_cmp_gt_u32 vcc, s62, v249\n\t"
    "s_cbranch_vccnz .Lst11%=\n\t"
    "s_branch .Lrt11%=\n\t"
    ".Lendt%=:\n\t"
    :: [rf] "s"(rf), [wb] "s"(wb), [l16] "v"(lane * 16), [xa0] "v"(xa0), [wa] "v"(wa), [fl4] "v"(lb + LDS_F + (lane & 3) * 4), [fz] "v"(lb + LDS_F), [own] "v"(lb + LDS_F + w2 * 4)
    : "memory", "vcc", "scc", "s60", "s61", "s62", "s63", "s64", "s65", "v12", "v13", "v14", "v15", "v16", "v17", "v18", "v19", "v20", "v21", "v22", "v23", "v24", "v25", "v26", "v27", "v28", "v29", "v30", "v31", "v32", "v33", "v34", "v35", "v36", "v37", "v38", "v39", "v40", "v41", "v42", "v43", "v44", "v45", "v46", "v47", "v48", "v49", "v50", "v51", "v52", "v53", "v54", "v55", "v56", "v57", "v58", "v59", "v60", "v61", "v62", "v63", "v64", "v65", "v66", "v67", "v68", "v69", "v70", "v71", "v72", "v73", "v74", "v75", "v76", "v77", "v78", "v79", "v80", "v81", "v82", "v83", "v84", "v85", "v86", "v87", "v88", "v89", "v90", "v91", "v92", "v93", "v94", "v95", "v96", "v97", "v98", "v99", "v100", "v101", "v102", "v103", "v104", "v105", "v106", "v107", "v108", "v109", "v110", "v111", "v112", "v113", "v114", "v115", "v116", "v117", "v118", "v119", "v120", "v121", "v122", "v123", "v124", "v125", "v126", "v127", "v128", "v129", "v130", "v131", "v132", "v133", "v134", "v135", "v136", "v137", "v138", "v139", "v140", "v141", "v142", "v143", "v144", "v145", "v146", "v147", "v148", "v149", "v150", "v151", "v152", "v153", "v154", "v155", "v156", "v157", "v158", "v159", "v160", "v161", "v162", "v163", "v164", "v165", "v166", "v167", "v168", "v169", "v170", "v171", "v172", "v173", "v174", "v175", "v176", "v177", "v178", "v179", "v180", "v181", "v182", "v183", "v184", "v185", "v186", "v187", "v188", "v189", "v190", "v191", "v192", "v193", "v194", "v195", "v196", "v197", "v198", "v199", "v200", "v201", "v202", "v203", "v204", "v205", "v206", "v207", "v208", "v209", "v210", "v211", "v212", "v213", "v214", "v215", "v216", "v217", "v218", "v219", "v220", "v221", "v222", "v223", "v224", "v225", "v226", "v227", "v228", "v229", "v230", "v231", "v232", "v233", "v234", "v235", "v236", "v237", "v238", "v239", "v240", "v241", "v242", "v243", "v244", "v245", "v246", "v247", "v248", "v249", "v250", "v251", "v252", "v253", "v254", "v255");
  }
}

DI void attn_wave(char* lds, const char* __restrict__ x, char* __restrict__ out, const int a, const int lane) {
  const int r16 = lane & 15, g = lane >> 4;
  auto rx = __builtin_amdgcn_make_buffer_rsrc((void*)x, (short)0, (int)XBYTES, 0x00020000);
  auto ro = __builtin_amdgcn_make_buffer_rsrc((void*)out, (short)0, (int)XBYTES, 0x00020000);
  const unsigned ld0 = 2 * a * ROWB + lane * 16, ld1 = ld0 + ROWB;
  const int b = blockIdx.x;
  unsigned b0 = win_base(b), b1 = win_base(b + 256), b2 = win_base(b + 512), sj = 0;
  u32x4 p0[6];
  #pragma unroll
  for (int m = 0; m < 6; ++m) p0[m] = __builtin_amdgcn_raw_buffer_load_b128(rx, (m < 3 ? ld0 : ld1) + (m % 3) * 1024, b0, 2);
  SB();
  asm volatile("s_barrier" ::: "memory");
  SB();
  const unsigned lb = (unsigned)(size_t)(lds_vu32*)lds;
  unsigned xw[6];
  #pragma unroll
  for (int p = 0; p < 2; ++p)
    #pragma unroll
    for (int m = 0; m < 3; ++m) {
      const int n = 64 * m + lane, tx = n / 24, c = n % 24, tok = 8 * (2 * a + p) + tx;
      xw[3 * p + m] = LDS_X + (c >> 3) * 8192 + tok * 128 + (((c & 7) ^ ((tok >> 1) & 7)) << 4);
    }
  const unsigned xr0 = lb + LDS_X + r16 * 128 + ((g ^ ((r16 >> 1) & 7)) << 4), xr1 = xr0 ^ 64;
  const unsigned ta = lb + LDS_T + a * 6144 + lane * 16, va = lb + LDS_V + lane * 16;
  const unsigned so4 = (2 * a + (g >> 1)) * ROWB + 4 * (g & 1) * 384 + 16 * r16, so2 = (2 * a + (g >> 1)) * ROWB + 4 * (g & 1) * 384 + 256 + 8 * r16;
  const unsigned chk1 = lb + ((lane & 4) ? F_T + 8 * a + 4 * (lane & 1) : F_S + 4 * (lane & 3)), chk2 = lb + ((lane & 4) ? F_V + 4 * (lane & 1) : F_X + 4 * (lane & 3));
  #pragma unroll
  for (int m = 0; m < 6; ++m) *(u32x4*)(lds + xw[m]) = p0[m];
  MEMFENCE(); *(lds_vu32*)(lds + F_X + a * 4) = 1; MEMFENCE();
  asm volatile(
    "v_mov_b32 v28, 2\n\t"
    "v_xor_b32 v29, 0x6000, %[va]\n\t"
    "buffer_load_dwordx4 v[56:59], %[ld0], %[rx], %[b1] offen nt\n\t"
    "buffer_load_dwordx4 v[60:63], %[ld0], %[rx], %[b1] offen offset:1024 nt\n\t"
    "buffer_load_dwordx4 v[64:67], %[ld0], %[rx], %[b1] offen offset:2048 nt\n\t"
    "buffer_load_dwordx4 v[68:71], %[ld1], %[rx], %[b1] offen nt\n\t"
    "buffer_load_dwordx4 v[72:75], %[ld1], %[rx], %[b1] offen offset:1024 nt\n\t"
    "buffer_load_dwordx4 v[76:79], %[ld1], %[rx], %[b1] offen offset:2048 nt\n\t"
    "s_waitcnt vmcnt(0)\n\t"
    "ds_write_b128 %[xw0], v[56:59] offset:24576\n\t"
    "ds_write_b128 %[xw1], v[60:63] offset:24576\n\t"
    "ds_write_b128 %[xw2], v[64:67] offset:24576\n\t"
    "ds_write_b128 %[xw3], v[68:71] offset:24576\n\t"
    "ds_write_b128 %[xw4], v[72:75] offset:24576\n\t"
    "ds_write_b128 %[xw5], v[76:79] offset:24576\n\t"
    "ds_write_b32 %[own], v28 offset:0\n\t"
    "buffer_load_dwordx4 v[80:83], %[ld0], %[rx], %[b2] offen nt\n\t"
    "buffer_load_dwordx4 v[84:87], %[ld0], %[rx], %[b2] offen offset:1024 nt\n\t"
    "buffer_load_dwordx4 v[88:91], %[ld0], %[rx], %[b2] offen offset:2048 nt\n\t"
    "buffer_load_dwordx4 v[92:95], %[ld1], %[rx], %[b2] offen nt\n\t"
    "buffer_load_dwordx4 v[96:99], %[ld1], %[rx], %[b2] offen offset:1024 nt\n\t"
    "buffer_load_dwordx4 v[100:103], %[ld1], %[rx], %[b2] offen offset:2048 nt\n\t"
    "s_mov_b32 s61, 1\n\t"
    "ds_read_b32 v31, %[chk2]\n\t"
    "s_waitcnt lgkmcnt(0)\n\t"
    "v_cmp_gt_u32 vcc, s61, v31\n\t"
    "s_cbranch_vccnz .Lsa1%=\n\t"
    ".Lra1%=:\n\t"
    "ds_read_b128 v[200:203], %[xr0] offset:0\n\t"
    "ds_read_b128 v[204:207], %[xr0] offset:2048\n\t"
    "ds_read_b128 v[208:211], %[xr0] offset:4096\n\t"
    "ds_read_b128 v[212:215], %[xr0] offset:6144\n\t"
    "ds_read_b128 v[216:219], %[xr1] offset:0\n\t"
    "ds_read_b128 v[220:223], %[xr1] offset:2048\n\t"
    "ds_read_b128 v[224:227], %[xr1] offset:4096\n\t"
    "ds_read_b128 v[228:231], %[xr1] offset:6144\n\t"
    "ds_read_b128 v[232:235], %[xr0] offset:8192\n\t"
    "ds_read_b128 v[236:239], %[xr0] offset:10240\n\t"
    "ds_read_b128 v[240:243], %[xr0] offset:12288\n\t"
    "ds_read_b128 v[244:247], %[xr0] offset:14336\n\t"
    "ds_read_b32 v30, %[chk1]\n\t"
    ".Lloopa%=:\n\t"
    "s_add_u32 s61, %[sj], 2\n\t"
    "v_add_u32 v28, 1, v28\n\t"
    "v_xor_b32 v29, 0x6000, v29\n\t"
    "s_add_u32 s60, %[b0], 18874368\n\t"
    "s_waitcnt lgkmcnt(0)\n\t"
    "v_cmp_gt_u32 vcc, s61, v30\n\t"
    "s_cbranch_vccnz .Lsa2%=\n\t"
    ".Lra2%=:\n\t"
    "ds_read_b128 v[176:179], %[ta] offset:0\n\t"
    "ds_read_b128 v[180:183], %[ta] offset:1024\n\t"
    "ds_read_b128 v[184:187], %[ta] offset:2048\n\t"
    "ds_read_b128 v[188:191], %[ta] offset:3072\n\t"
    "ds_read_b128 v[192:195], %[ta] offset:4096\n\t"
    "ds_read_b128 v[196:199], %[ta] offset:5120\n\t"
    "s_cmp_eq_u32 %[sj], 0\n\t"
    "s_cbranch_scc0 .Lv0%=\n\t"
    "s_waitcnt vmcnt(0)\n\t"
    ".Lv0%=:\n\t"
    "s_waitcnt vmcnt(8)\n\t"
    "ds_write_b128 %[xw0], v[80:83] offset:49152\n\t"
    "ds_write_b128 %[xw1], v[84:87] offset:49152\n\t"
    "ds_write_b128 %[xw2], v[88:91] offset:49152\n\t"
    "ds_write_b128 %[xw3], v[92:95] offset:49152\n\t"
    "ds_write_b128 %[xw4], v[96:99] offset:49152\n\t"
    "ds_write_b128 %[xw5], v[100:103] offset:49152\n\t"
    "ds_write_b32 %[own], v28 offset:0\n\t"
    "buffer_load_dwordx4 v[32:35], %[ld0], %[rx], s60 offen nt\n\t"
    "buffer_load_dwordx4 v[36:39], %[ld0], %[rx], s60 offen offset:1024 nt\n\t"
    "buffer_load_dwordx4 v[40:43], %[ld0], %[rx], s60 offen offset:2048 nt\n\t"
    "buffer_load_dwordx4 v[44:47], %[ld1], %[rx], s60 offen nt\n\t"
    "buffer_load_dwordx4 v[48:51], %[ld1], %[rx], s60 offen offset:1024 nt\n\t"
    "buffer_load_dwordx4 v[52:55], %[ld1], %[rx], s60 offen offset:2048 nt\n\t"
    "s_setprio 2\n\t"
    "s_waitcnt lgkmcnt(12)\n\t"
    "v_mfma_f32_16x16x32_bf16 v[152:155], v[200:203], v[176:179], 0\n\t"
    "ds_read_b128 v[200:203], %[xr1] offset:8192\n\t"
    "v_mfma_f32_16x16x32_bf16 v[156:159], v[204:207], v[176:179], 0\n\t"
    "ds_read_b128 v[204:207], %[xr1] offset:10240\n\t"
    "v_mfma_f32_16x16x32_bf16 v[160:163], v[208:211], v[176:179], 0\n\t"
    "ds_read_b128 v[208:211], %[xr1] offset:12288\n\t"
    "v_mfma_f32_16x16x32_bf16 v[164:167], v[212:215], v[176:179], 0\n\t"
    "ds_read_b128 v[212:215], %[xr1] offset:14336\n\t"
    "s_waitcnt lgkmcnt(11)\n\t"
    "v_mfma_f32_16x16x32_bf16 v[152:155], v[216:219], v[180:183], v[152:155]\n\t"
    "ds_read_b128 v[216:219], %[xr0] offset:16384\n\t"
    "v_mfma_f32_16x16x32_bf16 v[156:159], v[220:223], v[180:183], v[156:159]\n\t"
    "ds_read_b128 v[220:223], %[xr0] offset:18432\n\t"
    "v_mfma_f32_16x16x32_bf16 v[160:163], v[224:227], v[180:183], v[160:163]\n\t"
    "ds_read_b128 v[224:227], %[xr0] offset:20480\n\t"
    "v_mfma_f32_16x16x32_bf16 v[164:167], v[228:231], v[180:183], v[164:167]\n\t"
    "ds_read_b128 v[228:231], %[xr0] offset:22528\n\t"
    "v_mfma_f32_16x16x32_bf16 v[152:155], v[232:235], v[184:187], v[152:155]\n\t"
    "ds_read_b128 v[232:235], %[xr1] offset:16384\n\t"
    "v_mfma_f32_16x16x32_bf16 v[156:159], v[236:239], v[184:187], v[156:159]\n\t"
    "ds_read_b128 v[236:239], %[xr1] offset:18432\n\t"
    "v_mfma_f32_16x16x32_bf16 v[160:163], v[240:243], v[184:187], v[160:163]\n\t"
    "ds_read_b128 v[240:243], %[xr1] offset:20480\n\t"
    "v_mfma_f32_16x16x32_bf16 v[164:167], v[244:247], v[184:187], v[164:167]\n\t"
    "ds_read_b128 v[244:247], %[xr1] offset:22528\n\t"
    "s_waitcnt lgkmcnt(8)\n\t"
    "v_mfma_f32_16x16x32_bf16 v[152:155], v[200:203], v[188:191], v[152:155]\n\t"
    "v_mfma_f32_16x16x32_bf16 v[156:159], v[204:207], v[188:191], v[156:159]\n\t"
    "v_mfma_f32_16x16x32_bf16 v[160:163], v[208:211], v[188:191], v[160:163]\n\t"
    "v_mfma_f32_16x16x32_bf16 v[164:167], v[212:215], v[188:191], v[164:167]\n\t"
    "s_waitcnt lgkmcnt(4)\n\t"
    "v_mfma_f32_16x16x32_bf16 v[152:155], v[216:219], v[192:195], v[152:155]\n\t"
    "v_mfma_f32_16x16x32_bf16 v[156:159], v[220:223], v[192:195], v[156:159]\n\t"
    "v_mfma_f32_16x16x32_bf16 v[160:163], v[224:227], v[192:195], v[160:163]\n\t"
    "v_mfma_f32_16x16x32_bf16 v[164:167], v[228:231], v[192:195], v[164:167]\n\t"
    "s_waitcnt lgkmcnt(0)\n\t"
    "v_mfma_f32_16x16x32_bf16 v[152:155], v[232:235], v[196:199], v[152:155]\n\t"
    "v_mfma_f32_16x16x32_bf16 v[156:159], v[236:239], v[196:199], v[156:159]\n\t"
    "v_mfma_f32_16x16x32_bf16 v[160:163], v[240:243], v[196:199], v[160:163]\n\t"
    "v_mfma_f32_16x16x32_bf16 v[164:167], v[244:247], v[196:199], v[164:167]\n\t"
    "s_setprio 0\n\t"
    "ds_write_b32 %[own], v28 offset:16\n\t"
    "s_nop 2\n\t"
    "v_max3_f32 v176, v152, v153, v154\n\t"
    "v_max3_f32 v177, v156, v157, v158\n\t"
    "v_max3_f32 v176, v176, v155, v159\n\t"
    "v_max3_f32 v178, v160, v161, v162\n\t"
    "v_max3_f32 v177, v177, v163, v164\n\t"
    "v_max3_f32 v178, v178, v165, v166\n\t"
    "v_max3_f32 v176, v176, v177, v167\n\t"
    "v_max_f32 v180, v176, v178\n\t"
    "v_mov_b32 v181, v180\n\t"
    "s_nop 1\n\t"
    "v_permlane16_swap_b32 v180, v181\n\t"
    "v_max_f32 v180, v180, v181\n\t"
    "v_mov_b32 v181, v180\n\t"
    "s_nop 1\n\t"
    "v_permlane32_swap_b32 v180, v181\n\t"
    "v_max_f32 v180, v180, v181\n\t"
    "v_sub_f32 v152, v152, v180\n\t"
    "v_sub_f32 v153, v153, v180\n\t"
    "v_exp_f32 v152, v152\n\t"
    "v_exp_f32 v153, v153\n\t"
    "v_sub_f32 v154, v154, v180\n\t"
    "v_sub_f32 v155, v155, v180\n\t"
    "v_exp_f32 v154, v154\n\t"
    "v_exp_f32 v155, v155\n\t"
    "v_sub_f32 v156, v156, v180\n\t"
    "v_sub_f32 v157, v157, v180\n\t"
    "v_exp_f32 v156, v156\n\t"
    "v_exp_f32 v157, v157\n\t"
    "v_add_f32 v182, v152, v154\n\t"
    "v_add_f32 v183, v153, v155\n\t"
    "v_sub_f32 v158, v158, v180\n\t"
    "v_sub_f32 v159, v159, v180\n\t"
    "v_exp_f32 v158, v158\n\t"
    "v_exp_f32 v159, v159\n\t"
    "v_add_f32 v182, v182, v156\n\t"
    "v_add_f32 v183, v183, v157\n\t"
    "v_sub_f32 v160, v160, v180\n\t"
    "v_sub_f32 v161, v161, v180\n\t"
    "v_exp_f32 v160, v160\n\t"
    "v_exp_f32 v161, v161\n\t"
    "v_add_f32 v182, v182, v158\n\t"
    "v_add_f32 v183, v183, v159\n\t"
    "v_sub_f32 v162, v162, v180\n\t"
    "v_sub_f32 v163, v163, v180\n\t"
    "v_exp_f32 v162, v162\n\t"
    "v_exp_f32 v163, v163\n\t"
    "v_add_f32 v182, v182, v160\n\t"
    "v_add_f32 v183, v183, v161\n\t"
    "v_sub_f32 v164, v164, v180\n\t"
    "v_sub_f32 v165, v165, v180\n\t"
    "v_exp_f32 v164, v164\n\t"
    "v_exp_f32 v165, v165\n\t"
    "v_add_f32 v182, v182, v162\n\t"
    "v_add_f32 v183, v183, v163\n\t"
    "v_sub_f32 v166, v166, v180\n\t"
    "v_sub_f32 v167, v167, v180\n\t"
    "v_exp_f32 v166, v166\n\t"
    "v_exp_f32 v167, v167\n\t"
    "v_add_f32 v182, v182, v164\n\t"
    "v_add_f32 v183, v183, v165\n\t"
    "ds_read_b32 v31, %[chk2]\n\t"
    "v_add_f32 v182, v182, v166\n\t"
    "v_add_f32 v183, v183, v167\n\t"
    "v_add_f32 v182, v182, v183\n\t"
    "v_mov_b32 v181, v182\n\t"
    "s_nop 1\n\t"
    "v_permlane16_swap_b32 v182, v181\n\t"
    "v_add_f32 v182, v182, v181\n\t"
    "v_mov_b32 v181, v182\n\t"
    "s_nop 1\n\t"
    "v_permlane32_swap_b32 v182, v181\n\t"
    "v_add_f32 v182, v182, v181\n\t"
    "v_rcp_f32 v182, v182\n\t"
    "s_waitcnt lgkmcnt(0)\n\t"
    "v_cmp_gt_u32 vcc, s61, v31\n\t"
    "s_cbranch_vccnz .Lsa3%=\n\t"
    ".Lra3%=:\n\t"
    "ds_read_b128 v[200:203], v29 offset:0\n\t"
    "v_mul_f32 v152, v152, v182\n\t"
    "ds_read_b128 v[204:207], v29 offset:2048\n\t"
    "v_mul_f32 v153, v153, v182\n\t"
    "ds_read_b128 v[208:211], v29 offset:4096\n\t"
    "v_mul_f32 v154, v154, v182\n\t"
    "ds_read_b128 v[212:215], v29 offset:6144\n\t"
    "v_mul_f32 v155, v155, v182\n\t"
    "ds_read_b128 v[216:219], v29 offset:8192\n\t"
    "v_mul_f32 v156, v156, v182\n\t"
    "ds_read_b128 v[220:223], v29 offset:10240\n\t"
    "v_mul_f32 v157, v157, v182\n\t"
    "ds_read_b128 v[224:227], v29 offset:12288\n\t"
    "v_mul_f32 v158, v158, v182\n\t"
    "ds_read_b128 v[228:231], v29 offset:14336\n\t"
    "v_mul_f32 v159, v159, v182\n\t"
    "ds_read_b128 v[232:235], v29 offset:16384\n\t"
    "v_cvt_pk_bf16_f32 v168, v152, v153\n\t"
    "ds_read_b128 v[236:239], v29 offset:18432\n\t"
    "v_cvt_pk_bf16_f32 v169, v154, v155\n\t"
    "ds_read_b128 v[240:243], v29 offset:20480\n\t"
    "v_cvt_pk_bf16_f32 v170, v156, v157\n\t"
    "ds_read_b128 v[244:247], v29 offset:22528\n\t"
    "v_cvt_pk_bf16_f32 v171, v158, v159\n\t"
    "s_setprio 2\n\t"
    "s_waitcnt lgkmcnt(8)\n\t"
    "v_mfma_f32_16x16x32_bf16 v[104:107], v[168:171], v[200:203], 0\n\t"
    "v_mul_f32 v160, v160, v182\n\t"
    "ds_read_b128 v[200:203], v29 offset:1024\n\t"
    "v_mfma_f32_16x16x32_bf16 v[108:111], v[168:171], v[204:207], 0\n\t"
    "v_mul_f32 v161, v161, v182\n\t"
    "ds_read_b128 v[204:207], v29 offset:3072\n\t"
    "v_mfma_f32_16x16x32_bf16 v[112:115], v[168:171], v[208:211], 0\n\t"
    "v_mul_f32 v162, v162, v182\n\t"
    "ds_read_b128 v[208:211], v29 offset:5120\n\t"
    "v_mfma_f32_16x16x32_bf16 v[116:119], v[168:171], v[212:215], 0\n\t"
    "v_mul_f32 v163, v163, v182\n\t"
    "ds_read_b128 v[212:215], v29 offset:7168\n\t"
    "s_waitcnt lgkmcnt(8)\n\t"
    "v_mfma_f32_16x16x32_bf16 v[120:123], v[168:171], v[216:219], 0\n\t"
    "v_mul_f32 v164, v164, v182\n\t"
    "ds_read_b128 v[216:219], v29 offset:9216\n\t"
    "v_mfma_f32_16x16x32_bf16 v[124:127], v[168:171], v[220:223], 0\n\t"
    "v_mul_f32 v165, v165, v182\n\t"
    "ds_read_b128 v[220:223], v29 offset:11264\n\t"
    "v_mfma_f32_16x16x32_bf16 v[128:131], v[168:171], v[224:227], 0\n\t"
    "v_mul_f32 v166, v166, v182\n\t"
    "ds_read_b128 v[224:227], v29 offset:13312\n\t"
    "v_mfma_f32_16x16x32_bf16 v[132:135], v[168:171], v[228:231], 0\n\t"
    "v_mul_f32 v167, v167, v182\n\t"
    "ds_read_b128 v[228:231], v29 offset:15360\n\t"
    "s_waitcnt lgkmcnt(8)\n\t"
    "v_mfma_f32_16x16x32_bf16 v[136:139], v[168:171], v[232:235], 0\n\t"
    "v_cvt_pk_bf16_f32 v172, v160, v161\n\t"
    "v_cvt_pk_bf16_f32 v173, v162, v163\n\t"
    "ds_read_b128 v[232:235], v29 offset:17408\n\t"
    "v_mfma_f32_16x16x32_bf16 v[140:143], v[168:171], v[236:239], 0\n\t"
    "v_cvt_pk_bf16_f32 v174, v164, v165\n\t"
    "v_cvt_pk_bf16_f32 v175, v166, v167\n\t"
    "ds_read_b128 v[236:239], v29 offset:19456\n\t"
    "v_mfma_f32_16x16x32_bf16 v[144:147], v[168:171], v[240:243], 0\n\t"
    "ds_read_b128 v[240:243], v29 offset:21504\n\t"
    "v_mfma_f32_16x16x32_bf16 v[148:151], v[168:171], v[244:247], 0\n\t"
    "ds_read_b128 v[244:247], v29 offset:23552\n\t"
    "s_waitcnt lgkmcnt(8)\n\t"
    "v_mfma_f32_16x16x32_bf16 v[104:107], v[172:175], v[200:203], v[104:107]\n\t"
    "ds_read_b128 v[200:203], %[xr0] offset:24576\n\t"
    "v_mfma_f32_16x16x32_bf16 v[108:111], v[172:175], v[204:207], v[108:111]\n\t"
    "ds_read_b128 v[204:207], %[xr0] offset:26624\n\t"
    "v_mfma_f32_16x16x32_bf16 v[112:115], v[172:175], v[208:211], v[112:115]\n\t"
    "ds_read_b128 v[208:211], %[xr0] offset:28672\n\t"
    "v_mfma_f32_16x16x32_bf16 v[116:119], v[172:175], v[212:215], v[116:119]\n\t"
    "ds_read_b128 v[212:215], %[xr0] offset:30720\n\t"
    "s_waitcnt lgkmcnt(8)\n\t"
    "v_mfma_f32_16x16x32_bf16 v[120:123], v[172:175], v[216:219], v[120:123]\n\t"
    "ds_read_b128 v[216:219], %[xr1] offset:24576\n\t"
    "v_mfma_f32_16x16x32_bf16 v[124:127], v[172:175], v[220:223], v[124:127]\n\t"
    "ds_read_b128 v[220:223], %[xr1] offset:26624\n\t"
    "v_mfma_f32_16x16x32_bf16 v[128:131], v[172:175], v[224:227], v[128:131]\n\t"
    "ds_read_b128 v[224:227], %[xr1] offset:28672\n\t"
    "v_mfma_f32_16x16x32_bf16 v[132:135], v[172:175], v[228:231], v[132:135]\n\t"
    "ds_read_b128 v[228:231], %[xr1] offset:30720\n\t"
    "s_waitcnt lgkmcnt(8)\n\t"
    "ds_write_b32 %[own], v28 offset:32\n\t"
    "v_mfma_f32_16x16x32_bf16 v[136:139], v[172:175], v[232:235], v[136:139]\n\t"
    "ds_read_b128 v[232:235], %[xr0] offset:32768\n\t"
    "v_mfma_f32_16x16x32_bf16 v[140:143], v[172:175], v[236:239], v[140:143]\n\t"
    "ds_read_b128 v[236:239], %[xr0] offset:34816\n\t"
    "v_mfma_f32_16x16x32_bf16 v[144:147], v[172:175], v[240:243], v[144:147]\n\t"
    "ds_read_b128 v[240:243], %[xr0] offset:36864\n\t"
    "v_mfma_f32_16x16x32_bf16 v[148:151], v[172:175], v[244:247], v[148:151]\n\t"
    "ds_read_b128 v[244:247], %[xr0] offset:38912\n\t"
    "s_setprio 0\n\t"
    "v_cvt_pk_bf16_f32 v248, v104, v108\n\t"
    "v_cvt_pk_bf16_f32 v249, v112, v116\n\t"
    "v_cvt_pk_bf16_f32 v250, v120, v124\n\t"
    "v_cvt_pk_bf16_f32 v251, v128, v132\n\t"
    "v_cvt_pk_bf16_f32 v252, v136, v140\n\t"
    "s_nop 0\n\t"
    "v_cvt_pk_bf16_f32 v253, v144, v148\n\t"
    "buffer_store_dwordx4 v[248:251], %[so4], %[ro], %[b0] offen sc1\n\t"
    "buffer_store_dwordx2 v[252:253], %[so2], %[ro], %[b0] offen sc1\n\t"
    "s_nop 0\n\t"
    "v_cvt_pk_bf16_f32 v248, v105, v109\n\t"
    "v_cvt_pk_bf16_f32 v249, v113, v117\n\t"
    "v_cvt_pk_bf16_f32 v250, v121, v125\n\t"
    "v_cvt_pk_bf16_f32 v251, v129, v133\n\t"
    "v_cvt_pk_bf16_f32 v252, v137, v141\n\t"
    "v_cvt_pk_bf16_f32 v253, v145, v149\n\t"
    "buffer_store_dwordx4 v[248:251], %[so4], %[ro], %[b0] offen offset:384 sc1\n\t"
    "buffer_store_dwordx2 v[252:253], %[so2], %[ro], %[b0] offen offset:384 sc1\n\t"
    "s_nop 0\n\t"
    "v_cvt_pk_bf16_f32 v248, v106, v110\n\t"
    "v_cvt_pk_bf16_f32 v249, v114, v118\n\t"
    "v_cvt_pk_bf16_f32 v250, v122, v126\n\t"
    "v_cvt_pk_bf16_f32 v251, v130, v134\n\t"
    "v_cvt_pk_bf16_f32 v252, v138, v142\n\t"
    "v_cvt_pk_bf16_f32 v253, v146, v150\n\t"
    "buffer_store_dwordx4 v[248:251], %[so4], %[ro], %[b0] offen offset:768 sc1\n\t"
    "buffer_store_dwordx2 v[252:253], %[so2], %[ro], %[b0] offen offset:768 sc1\n\t"
    "ds_read_b32 v30, %[chk1]\n\t"
    "v_cvt_pk_bf16_f32 v248, v107, v111\n\t"
    "v_cvt_pk_bf16_f32 v249, v115, v119\n\t"
    "v_cvt_pk_bf16_f32 v250, v123, v127\n\t"
    "v_cvt_pk_bf16_f32 v251, v131, v135\n\t"
    "v_cvt_pk_bf16_f32 v252, v139, v143\n\t"
    "v_cvt_pk_bf16_f32 v253, v147, v151\n\t"
    "buffer_store_dwordx4 v[248:251], %[so4], %[ro], %[b0] offen offset:1152 sc1\n\t"
    "buffer_store_dwordx2 v[252:253], %[so2], %[ro], %[b0] offen offset:1152 sc1\n\t"
    "s_add_u32 s61, %[sj], 3\n\t"
    "v_add_u32 v28, 1, v28\n\t"
    "v_xor_b32 v29, 0x6000, v29\n\t"
    "s_add_u32 s60, %[b1], 18874368\n\t"
    "s_waitcnt lgkmcnt(0)\n\t"
    "v_cmp_gt_u32 vcc, s61, v30\n\t"
    "s_cbranch_vccnz .Lsa4%=\n\t"
    ".Lra4%=:\n\t"
    "ds_read_b128 v[176:179], %[ta] offset:0\n\t"
    "ds_read_b128 v[180:183], %[ta] offset:1024\n\t"
    "ds_read_b128 v[184:187], %[ta] offset:2048\n\t"
    "ds_read_b128 v[188:191], %[ta] offset:3072\n\t"
    "ds_read_b128 v[192:195], %[ta] offset:4096\n\t"
    "ds_read_b128 v[196:199], %[ta] offset:5120\n\t"
    "s_waitcnt vmcnt(8)\n\t"
    "ds_write_b128 %[xw0], v[32:35]\n\t"
    "ds_write_b128 %[xw1], v[36:39]\n\t"
    "ds_write_b128 %[xw2], v[40:43]\n\t"
    "ds_write_b128 %[xw3], v[44:47]\n\t"
    "ds_write_b128 %[xw4], v[48:51]\n\t"
    "ds_write_b128 %[xw5], v[52:55]\n\t"
    "ds_write_b32 %[own], v28 offset:0\n\t"
    "buffer_load_dwordx4 v[56:59], %[ld0], %[rx], s60 offen nt\n\t"
    "buffer_load_dwordx4 v[60:63], %[ld0], %[rx], s60 offen offset:1024 nt\n\t"
    "buffer_load_dwordx4 v[64:67], %[ld0], %[rx], s60 offen offset:2048 nt\n\t"
    "buffer_load_dwordx4 v[68:71], %[ld1], %[rx], s60 offen nt\n\t"
    "buffer_load_dwordx4 v[72:75], %[ld1], %[rx], s60 offen offset:1024 nt\n\t"
    "buffer_load_dwordx4 v[76:79], %[ld1], %[rx], s60 offen offset:2048 nt\n\t"
    "s_setprio 2\n\t"
    "s_waitcnt lgkmcnt(12)\n\t"
    "v_mfma_f32_16x16x32_bf16 v[152:155], v[200:203], v[176:179], 0\n\t"
    "ds_read_b128 v[200:203], %[xr1] offset:32768\n\t"
    "v_mfma_f32_16x16x32_bf16 v[156:159], v[204:207], v[176:179], 0\n\t"
    "ds_read_b128 v[204:207], %[xr1] offset:34816\n\t"
    "v_mfma_f32_16x16x32_bf16 v[160:163], v[208:211], v[176:179], 0\n\t"
    "ds_read_b128 v[208:211], %[xr1] offset:36864\n\t"
    "v_mfma_f32_16x16x32_bf16 v[164:167], v[212:215], v[176:179], 0\n\t"
    "ds_read_b128 v[212:215], %[xr1] offset:38912\n\t"
    "s_waitcnt lgkmcnt(11)\n\t"
    "v_mfma_f32_16x16x32_bf16 v[152:155], v[216:219], v[180:183], v[152:155]\n\t"
    "ds_read_b128 v[216:219], %[xr0] offset:40960\n\t"
    "v_mfma_f32_16x16x32_bf16 v[156:159], v[220:223], v[180:183], v[156:159]\n\t"
    "ds_read_b128 v[220:223], %[xr0] offset:43008\n\t"
    "v_mfma_f32_16x16x32_bf16 v[160:163], v[224:227], v[180:183], v[160:163]\n\t"
    "ds_read_b128 v[224:227], %[xr0] offset:45056\n\t"
    "v_mfma_f32_16x16x32_bf16 v[164:167], v[228:231], v[180:183], v[164:167]\n\t"
    "ds_read_b128 v[228:231], %[xr0] offset:47104\n\t"
    "v_mfma_f32_16x16x32_bf16 v[152:155], v[232:235], v[184:187], v[152:155]\n\t"
    "ds_read_b128 v[232:235], %[xr1] offset:40960\n\t"
    "v_mfma_f32_16x16x32_bf16 v[156:159], v[236:239], v[184:187], v[156:159]\n\t"
    "ds_read_b128 v[236:239], %[xr1] offset:43008\n\t"
    "v_mfma_f32_16x16x32_bf16 v[160:163], v[240:243], v[184:187], v[160:163]\n\t"
    "ds_read_b128 v[240:243], %[xr1] offset:45056\n\t"
    "v_mfma_f32_16x16x32_bf16 v[164:167], v[244:247], v[184:187], v[164:167]\n\t"
    "ds_read_b128 v[244:247], %[xr1] offset:47104\n\t"
    "s_waitcnt lgkmcnt(8)\n\t"
    "v_mfma_f32_16x16x32_bf16 v[152:155], v[200:203], v[188:191], v[152:155]\n\t"
    "v_mfma_f32_16x16x32_bf16 v[156:159], v[204:207], v[188:191], v[156:159]\n\t"
    "v_mfma_f32_16x16x32_bf16 v[160:163], v[208:211], v[188:191], v[160:163]\n\t"
    "v_mfma_f32_16x16x32_bf16 v[164:167], v[212:215], v[188:191], v[164:167]\n\t"
    "s_waitcnt lgkmcnt(4)\n\t"
    "v_mfma_f32_16x16x32_bf16 v[152:155], v[216:219], v[192:195], v[152:155]\n\t"
    "v_mfma_f32_16x16x32_bf16 v[156:159], v[220:223], v[192:195], v[156:159]\n\t"
    "v_mfma_f32_16x16x32_bf16 v[160:163], v[224:227], v[192:195], v[160:163]\n\t"
    "v_mfma_f32_16x16x32_bf16 v[164:167], v[228:231], v[192:195], v[164:167]\n\t"
    "s_waitcnt lgkmcnt(0)\n\t"
    "v_mfma_f32_16x16x32_bf16 v[152:155], v[232:235], v[196:199], v[152:155]\n\t"
    "v_mfma_f32_16x16x32_bf16 v[156:159], v[236:239], v[196:199], v[156:159]\n\t"
    "v_mfma_f32_16x16x32_bf16 v[160:163], v[240:243], v[196:199], v[160:163]\n\t"
    "v_mfma_f32_16x16x32_bf16 v[164:167], v[244:247], v[196:199], v[164:167]\n\t"
    "s_setprio 0\n\t"
    "ds_write_b32 %[own], v28 offset:16\n\t"
    "s_nop 2\n\t"
    "v_max3_f32 v176, v152, v153, v154\n\t"
    "v_max3_f32 v177, v156, v157, v158\n\t"
    "v_max3_f32 v176, v176, v155, v159\n\t"
    "v_max3_f32 v178, v160, v161, v162\n\t"
    "v_max3_f32 v177, v177, v163, v164\n\t"
    "v_max3_f32 v178, v178, v165, v166\n\t"
    "v_max3_f32 v176, v176, v177, v167\n\t"
    "v_max_f32 v180, v176, v178\n\t"
    "v_mov_b32 v181, v180\n\t"
    "s_nop 1\n\t"
    "v_permlane16_swap_b32 v180, v181\n\t"
    "v_max_f32 v180, v180, v181\n\t"
    "v_mov_b32 v181, v180\n\t"
    "s_nop 1\n\t"
    "v_permlane32_swap_b32 v180, v181\n\t"
    "v_max_f32 v180, v180, v181\n\t"
    "v_sub_f32 v152, v152, v180\n\t"
    "v_sub_f32 v153, v153, v180\n\t"
    "v_exp_f32 v152, v152\n\t"
    "v_exp_f32 v153, v153\n\t"
    "v_sub_f32 v154, v154, v180\n\t"
    "v_sub_f32 v155, v155, v180\n\t"
    "v_exp_f32 v154, v154\n\t"
    "v_exp_f32 v155, v155\n\t"
    "v_sub_f32 v156, v156, v180\n\t"
    "v_sub_f32 v157, v157, v180\n\t"
    "v_exp_f32 v156, v156\n\t"
    "v_exp_f32 v157, v157\n\t"
    "v_add_f32 v182, v152, v154\n\t"
    "v_add_f32 v183, v153, v155\n\t"
    "v_sub_f32 v158, v158, v180\n\t"
    "v_sub_f32 v159, v159, v180\n\t"
    "v_exp_f32 v158, v158\n\t"
    "v_exp_f32 v159, v159\n\t"
    "v_add_f32 v182, v182, v156\n\t"
    "v_add_f32 v183, v183, v157\n\t"
    "v_sub_f32 v160, v160, v180\n\t"
    "v_sub_f32 v161, v161, v180\n\t"
    "v_exp_f32 v160, v160\n\t"
    "v_exp_f32 v161, v161\n\t"
    "v_add_f32 v182, v182, v158\n\t"
    "v_add_f32 v183, v183, v159\n\t"
    "v_sub_f32 v162, v162, v180\n\t"
    "v_sub_f32 v163, v163, v180\n\t"
    "v_exp_f32 v162, v162\n\t"
    "v_exp_f32 v163, v163\n\t"
    "v_add_f32 v182, v182, v160\n\t"
    "v_add_f32 v183, v183, v161\n\t"
    "v_sub_f32 v164, v164, v180\n\t"
    "v_sub_f32 v165, v165, v180\n\t"
    "v_exp_f32 v164, v164\n\t"
    "v_exp_f32 v165, v165\n\t"
    "v_add_f32 v182, v182, v162\n\t"
    "v_add_f32 v183, v183, v163\n\t"
    "v_sub_f32 v166, v166, v180\n\t"
    "v_sub_f32 v167, v167, v180\n\t"
    "v_exp_f32 v166, v166\n\t"
    "v_exp_f32 v167, v167\n\t"
    "v_add_f32 v182, v182, v164\n\t"
    "v_add_f32 v183, v183, v165\n\t"
    "ds_read_b32 v31, %[chk2]\n\t"
    "v_add_f32 v182, v182, v166\n\t"
    "v_add_f32 v183, v183, v167\n\t"
    "v_add_f32 v182, v182, v183\n\t"
    "v_mov_b32 v181, v182\n\t"
    "s_nop 1\n\t"
    "v_permlane16_swap_b32 v182, v181\n\t"
    "v_add_f32 v182, v182, v181\n\t"
    "v_mov_b32 v181, v182\n\t"
    "s_nop 1\n\t"
    "v_permlane32_swap_b32 v182, v181\n\t"
    "v_add_f32 v182, v182, v181\n\t"
    "v_rcp_f32 v182, v182\n\t"
    "s_waitcnt lgkmcnt(0)\n\t"
    "v_cmp_gt_u32 vcc, s61, v31\n\t"
    "s_cbranch_vccnz .Lsa5%=\n\t"
    ".Lra5%=:\n\t"
    "ds_read_b128 v[200:203], v29 offset:0\n\t"
    "v_mul_f32 v152, v152, v182\n\t"
    "ds_read_b128 v[204:207], v29 offset:2048\n\t"
    "v_mul_f32 v153, v153, v182\n\t"
    "ds_read_b128 v[208:211], v29 offset:4096\n\t"
    "v_mul_f32 v154, v154, v182\n\t"
    "ds_read_b128 v[212:215], v29 offset:6144\n\t"
    "v_mul_f32 v155, v155, v182\n\t"
    "ds_read_b128 v[216:219], v29 offset:8192\n\t"
    "v_mul_f32 v156, v156, v182\n\t"
    "ds_read_b128 v[220:223], v29 offset:10240\n\t"
    "v_mul_f32 v157, v157, v182\n\t"
    "ds_read_b128 v[224:227], v29 offset:12288\n\t"
    "v_mul_f32 v158, v158, v182\n\t"
    "ds_read_b128 v[228:231], v29 offset:14336\n\t"
    "v_mul_f32 v159, v159, v182\n\t"
    "ds_read_b128 v[232:235], v29 offset:16384\n\t"
    "v_cvt_pk_bf16_f32 v168, v152, v153\n\t"
    "ds_read_b128 v[236:239], v29 offset:18432\n\t"
    "v_cvt_pk_bf16_f32 v169, v154, v155\n\t"
    "ds_read_b128 v[240:243], v29 offset:20480\n\t"
    "v_cvt_pk_bf16_f32 v170, v156, v157\n\t"
    "ds_read_b128 v[244:247], v29 offset:22528\n\t"
    "v_cvt_pk_bf16_f32 v171, v158, v159\n\t"
    "s_setprio 2\n\t"
    "s_waitcnt lgkmcnt(8)\n\t"
    "v_mfma_f32_16x16x32_bf16 v[104:107], v[168:171], v[200:203], 0\n\t"
    "v_mul_f32 v160, v160, v182\n\t"
    "ds_read_b128 v[200:203], v29 offset:1024\n\t"
    "v_mfma_f32_16x16x32_bf16 v[108:111], v[168:171], v[204:207], 0\n\t"
    "v_mul_f32 v161, v161, v182\n\t"
    "ds_read_b128 v[204:207], v29 offset:3072\n\t"
    "v_mfma_f32_16x16x32_bf16 v[112:115], v[168:171], v[208:211], 0\n\t"
    "v_mul_f32 v162, v162, v182\n\t"
    "ds_read_b128 v[208:211], v29 offset:5120\n\t"
    "v_mfma_f32_16x16x32_bf16 v[116:119], v[168:171], v[212:215], 0\n\t"
    "v_mul_f32 v163, v163, v182\n\t"
    "ds_read_b128 v[212:215], v29 offset:7168\n\t"
    "s_waitcnt lgkmcnt(8)\n\t"
    "v_mfma_f32_16x16x32_bf16 v[120:123], v[168:171], v[216:219], 0\n\t"
    "v_mul_f32 v164, v164, v182\n\t"
    "ds_read_b128 v[216:219], v29 offset:9216\n\t"
    "v_mfma_f32_16x16x32_bf16 v[124:127], v[168:171], v[220:223], 0\n\t"
    "v_mul_f32 v165, v165, v182\n\t"
    "ds_read_b128 v[220:223], v29 offset:11264\n\t"
    "v_mfma_f32_16x16x32_bf16 v[128:131], v[168:171], v[224:227], 0\n\t"
    "v_mul_f32 v166, v166, v182\n\t"
    "ds_read_b128 v[224:227], v29 offset:13312\n\t"
    "v_mfma_f32_16x16x32_bf16 v[132:135], v[168:171], v[228:231], 0\n\t"
    "v_mul_f32 v167, v167, v182\n\t"
    "ds_read_b128 v[228:231], v29 offset:15360\n\t"
    "s_waitcnt lgkmcnt(8)\n\t"
    "v_mfma_f32_16x16x32_bf16 v[136:139], v[168:171], v[232:235], 0\n\t"
    "v_cvt_pk_bf16_f32 v172, v160, v161\n\t"
    "v_cvt_pk_bf16_f32 v173, v162, v163\n\t"
    "ds_read_b128 v[232:235], v29 offset:17408\n\t"
    "v_mfma_f32_16x16x32_bf16 v[140:143], v[168:171], v[236:239], 0\n\t"
    "v_cvt_pk_bf16_f32 v174, v164, v165\n\t"
    "v_cvt_pk_bf16_f32 v175, v166, v167\n\t"
    "ds_read_b128 v[236:239], v29 offset:19456\n\t"
    "v_mfma_f32_16x16x32_bf16 v[144:147], v[168:171], v[240:243], 0\n\t"
    "ds_read_b128 v[240:243], v29 offset:21504\n\t"
    "v_mfma_f32_16x16x32_bf16 v[148:151], v[168:171], v[244:247], 0\n\t"
    "ds_read_b128 v[244:247], v29 offset:23552\n\t"
    "s_waitcnt lgkmcnt(8)\n\t"
    "v_mfma_f32_16x16x32_bf16 v[104:107], v[172:175], v[200:203], v[104:107]\n\t"
    "ds_read_b128 v[200:203], %[xs0] offset:0\n\t"
    "v_mfma_f32_16x16x32_bf16 v[108:111], v[172:175], v[204:207], v[108:111]\n\t"
    "ds_read_b128 v[204:207], %[xs0] offset:2048\n\t"
    "v_mfma_f32_16x16x32_bf16 v[112:115], v[172:175], v[208:211], v[112:115]\n\t"
    "ds_read_b128 v[208:211], %[xs0] offset:4096\n\t"
    "v_mfma_f32_16x16x32_bf16 v[116:119], v[172:175], v[212:215], v[116:119]\n\t"
    "ds_read_b128 v[212:215], %[xs0] offset:6144\n\t"
    "s_waitcnt lgkmcnt(8)\n\t"
    "v_mfma_f32_16x16x32_bf16 v[120:123], v[172:175], v[216:219], v[120:123]\n\t"
    "ds_read_b128 v[216:219], %[xs1] offset:0\n\t"
    "v_mfma_f32_16x16x32_bf16 v[124:127], v[172:175], v[220:223], v[124:127]\n\t"
    "ds_read_b128 v[220:223], %[xs1] offset:2048\n\t"
    "v_mfma_f32_16x16x32_bf16 v[128:131], v[172:175], v[224:227], v[128:131]\n\t"
    "ds_read_b128 v[224:227], %[xs1] offset:4096\n\t"
    "v_mfma_f32_16x16x32_bf16 v[132:135], v[172:175], v[228:231], v[132:135]\n\t"
    "ds_read_b128 v[228:231], %[xs1] offset:6144\n\t"
    "s_waitcnt lgkmcnt(8)\n\t"
    "ds_write_b32 %[own], v28 offset:32\n\t"
    "v_mfma_f32_16x16x32_bf16 v[136:139], v[172:175], v[232:235], v[136:139]\n\t"
    "ds_read_b128 v[232:235], %[xs0] offset:8192\n\t"
    "v_mfma_f32_16x16x32_bf16 v[140:143], v[172:175], v[236:239], v[140:143]\n\t"
    "ds_read_b128 v[236:239], %[xs0] offset:10240\n\t"
    "v_mfma_f32_16x16x32_bf16 v[144:147], v[172:175], v[240:243], v[144:147]\n\t"
    "ds_read_b128 v[240:243], %[xs0] offset:12288\n\t"
    "v_mfma_f32_16x16x32_bf16 v[148:151], v[172:175], v[244:247], v[148:151]\n\t"
    "ds_read_b128 v[244:247], %[xs0] offset:14336\n\t"
    "s_setprio 0\n\t"
    "v_cvt_pk_bf16_f32 v248, v104, v108\n\t"
    "v_cvt_pk_bf16_f32 v249, v112, v116\n\t"
    "v_cvt_pk_bf16_f32 v250, v120, v124\n\t"
    "v_cvt_pk_bf16_f32 v251, v128, v132\n\t"
    "v_cvt_pk_bf16_f32 v252, v136, v140\n\t"
    "s_nop 0\n\t"
    "v_cvt_pk_bf16_f32 v253, v144, v148\n\t"
    "buffer_store_dwordx4 v[248:251], %[so4], %[ro], %[b1] offen sc1\n\t"
    "buffer_store_dwordx2 v[252:253], %[so2], %[ro], %[b1] offen sc1\n\t"
    "s_nop 0\n\t"
    "v_cvt_pk_bf16_f32 v248, v105, v109\n\t"
    "v_cvt_pk_bf16_f32 v249, v113, v117\n\t"
    "v_cvt_pk_bf16_f32 v250, v121, v125\n\t"
    "v_cvt_pk_bf16_f32 v251, v129, v133\n\t"
    "v_cvt_pk_bf16_f32 v252, v137, v141\n\t"
    "v_cvt_pk_bf16_f32 v253, v145, v149\n\t"
    "buffer_store_dwordx4 v[248:251], %[so4], %[ro], %[b1] offen offset:384 sc1\n\t"
    "buffer_store_dwordx2 v[252:253], %[so2], %[ro], %[b1] offen offset:384 sc1\n\t"
    "s_nop 0\n\t"
    "v_cvt_pk_bf16_f32 v248, v106, v110\n\t"
    "v_cvt_pk_bf16_f32 v249, v114, v118\n\t"
    "v_cvt_pk_bf16_f32 v250, v122, v126\n\t"
    "v_cvt_pk_bf16_f32 v251, v130, v134\n\t"
    "v_cvt_pk_bf16_f32 v252, v138, v142\n\t"
    "v_cvt_pk_bf16_f32 v253, v146, v150\n\t"
    "buffer_store_dwordx4 v[248:251], %[so4], %[ro], %[b1] offen offset:768 sc1\n\t"
    "buffer_store_dwordx2 v[252:253], %[so2], %[ro], %[b1] offen offset:768 sc1\n\t"
    "ds_read_b32 v30, %[chk1]\n\t"
    "v_cvt_pk_bf16_f32 v248, v107, v111\n\t"
    "v_cvt_pk_bf16_f32 v249, v115, v119\n\t"
    "v_cvt_pk_bf16_f32 v250, v123, v127\n\t"
    "v_cvt_pk_bf16_f32 v251, v131, v135\n\t"
    "v_cvt_pk_bf16_f32 v252, v139, v143\n\t"
    "v_cvt_pk_bf16_f32 v253, v147, v151\n\t"
    "buffer_store_dwordx4 v[248:251], %[so4], %[ro], %[b1] offen offset:1152 sc1\n\t"
    "buffer_store_dwordx2 v[252:253], %[so2], %[ro], %[b1] offen offset:1152 sc1\n\t"
    "s_add_u32 s61, %[sj], 4\n\t"
    "v_add_u32 v28, 1, v28\n\t"
    "v_xor_b32 v29, 0x6000, v29\n\t"
    "s_add_u32 s60, %[b2], 18874368\n\t"
    "s_waitcnt lgkmcnt(0)\n\t"
    "v_cmp_gt_u32 vcc, s61, v30\n\t"
    "s_cbranch_vccnz .Lsa6%=\n\t"
    ".Lra6%=:\n\t"
    "ds_read_b128 v[176:179], %[ta] offset:0\n\t"
    "ds_read_b128 v[180:183], %[ta] offset:1024\n\t"
    "ds_read_b128 v[184:187], %[ta] offset:2048\n\t"
    "ds_read_b128 v[188:191], %[ta] offset:3072\n\t"
    "ds_read_b128 v[192:195], %[ta] offset:4096\n\t"
    "ds_read_b128 v[196:199], %[ta] offset:5120\n\t"
    "s_waitcnt vmcnt(8)\n\t"
    "ds_write_b128 %[xw0], v[56:59] offset:24576\n\t"
    "ds_write_b128 %[xw1], v[60:63] offset:24576\n\t"
    "ds_write_b128 %[xw2], v[64:67] offset:24576\n\t"
    "ds_write_b128 %[xw3], v[68:71] offset:24576\n\t"
    "ds_write_b128 %[xw4], v[72:75] offset:24576\n\t"
    "ds_write_b128 %[xw5], v[76:79] offset:24576\n\t"
    "ds_write_b32 %[own], v28 offset:0\n\t"
    "buffer_load_dwordx4 v[80:83], %[ld0], %[rx], s60 offen nt\n\t"
    "buffer_load_dwordx4 v[84:87], %[ld0], %[rx], s60 offen offset:1024 nt\n\t"
    "buffer_load_dwordx4 v[88:91], %[ld0], %[rx], s60 offen offset:2048 nt\n\t"
    "buffer_load_dwordx4 v[92:95], %[ld1], %[rx], s60 offen nt\n\t"
    "buffer_load_dwordx4 v[96:99], %[ld1], %[rx], s60 offen offset:1024 nt\n\t"
    "buffer_load_dwordx4 v[100:103], %[ld1], %[rx], s60 offen offset:2048 nt\n\t"
    "s_setprio 2\n\t"
    "s_waitcnt lgkmcnt(12)\n\t"
    "v_mfma_f32_16x16x32_bf16 v[152:155], v[200:203], v[176:179], 0\n\t"
    "ds_read_b128 v[200:203], %[xs1] offset:8192\n\t"
    "v_mfma_f32_16x16x32_bf16 v[156:159], v[204:207], v[176:179], 0\n\t"
    "ds_read_b128 v[204:207], %[xs1] offset:10240\n\t"
    "v_mfma_f32_16x16x32_bf16 v[160:163], v[208:211], v[176:179], 0\n\t"
    "ds_read_b128 v[208:211], %[xs1] offset:12288\n\t"
    "v_mfma_f32_16x16x32_bf16 v[164:167], v[212:215], v[176:179], 0\n\t"
    "ds_read_b128 v[212:215], %[xs1] offset:14336\n\t"
    "s_waitcnt lgkmcnt(11)\n\t"
    "v_mfma_f32_16x16x32_bf16 v[152:155], v[216:219], v[180:183], v[152:155]\n\t"
    "ds_read_b128 v[216:219], %[xs0] offset:16384\n\t"
    "v_mfma_f32_16x16x32_bf16 v[156:159], v[220:223], v[180:183], v[156:159]\n\t"
    "ds_read_b128 v[220:223], %[xs0] offset:18432\n\t"
    "v_mfma_f32_16x16x32_bf16 v[160:163], v[224:227], v[180:183], v[160:163]\n\t"
    "ds_read_b128 v[224:227], %[xs0] offset:20480\n\t"
    "v_mfma_f32_16x16x32_bf16 v[164:167], v[228:231], v[180:183], v[164:167]\n\t"
    "ds_read_b128 v[228:231], %[xs0] offset:22528\n\t"
    "v_mfma_f32_16x16x32_bf16 v[152:155], v[232:235], v[184:187], v[152:155]\n\t"
    "ds_read_b128 v[232:235], %[xs1] offset:16384\n\t"
    "v_mfma_f32_16x16x32_bf16 v[156:159], v[236:239], v[184:187], v[156:159]\n\t"
    "ds_read_b128 v[236:239], %[xs1] offset:18432\n\t"
    "v_mfma_f32_16x16x32_bf16 v[160:163], v[240:243], v[184:187], v[160:163]\n\t"
    "ds_read_b128 v[240:243], %[xs1] offset:20480\n\t"
    "v_mfma_f32_16x16x32_bf16 v[164:167], v[244:247], v[184:187], v[164:167]\n\t"
    "ds_read_b128 v[244:247], %[xs1] offset:22528\n\t"
    "s_waitcnt lgkmcnt(8)\n\t"
    "v_mfma_f32_16x16x32_bf16 v[152:155], v[200:203], v[188:191], v[152:155]\n\t"
    "v_mfma_f32_16x16x32_bf16 v[156:159], v[204:207], v[188:191], v[156:159]\n\t"
    "v_mfma_f32_16x16x32_bf16 v[160:163], v[208:211], v[188:191], v[160:163]\n\t"
    "v_mfma_f32_16x16x32_bf16 v[164:167], v[212:215], v[188:191], v[164:167]\n\t"
    "s_waitcnt lgkmcnt(4)\n\t"
    "v_mfma_f32_16x16x32_bf16 v[152:155], v[216:219], v[192:195], v[152:155]\n\t"
    "v_mfma_f32_16x16x32_bf16 v[156:159], v[220:223], v[192:195], v[156:159]\n\t"
    "v_mfma_f32_16x16x32_bf16 v[160:163], v[224:227], v[192:195], v[160:163]\n\t"
    "v_mfma_f32_16x16x32_bf16 v[164:167], v[228:231], v[192:195], v[164:167]\n\t"
    "s_waitcnt lgkmcnt(0)\n\t"
    "v_mfma_f32_16x16x32_bf16 v[152:155], v[232:235], v[196:199], v[152:155]\n\t"
    "v_mfma_f32_16x16x32_bf16 v[156:159], v[236:239], v[196:199], v[156:159]\n\t"
    "v_mfma_f32_16x16x32_bf16 v[160:163], v[240:243], v[196:199], v[160:163]\n\t"
    "v_mfma_f32_16x16x32_bf16 v[164:167], v[244:247], v[196:199], v[164:167]\n\t"
    "s_setprio 0\n\t"
    "ds_write_b32 %[own], v28 offset:16\n\t"
    "s_nop 2\n\t"
    "v_max3_f32 v176, v152, v153, v154\n\t"
    "v_max3_f32 v177, v156, v157, v158\n\t"
    "v_max3_f32 v176, v176, v155, v159\n\t"
    "v_max3_f32 v178, v160, v161, v162\n\t"
    "v_max3_f32 v177, v177, v163, v164\n\t"
    "v_max3_f32 v178, v178, v165, v166\n\t"
    "v_max3_f32 v176, v176, v177, v167\n\t"
    "v_max_f32 v180, v176, v178\n\t"
    "v_mov_b32 v181, v180\n\t"
    "s_nop 1\n\t"
    "v_permlane16_swap_b32 v180, v181\n\t"
    "v_max_f32 v180, v180, v181\n\t"
    "v_mov_b32 v181, v180\n\t"
    "s_nop 1\n\t"
    "v_permlane32_swap_b32 v180, v181\n\t"
    "v_max_f32 v180, v180, v181\n\t"
    "v_sub_f32 v152, v152, v180\n\t"
    "v_sub_f32 v153, v153, v180\n\t"
    "v_exp_f32 v152, v152\n\t"
    "v_exp_f32 v153, v153\n\t"
    "v_sub_f32 v154, v154, v180\n\t"
    "v_sub_f32 v155, v155, v180\n\t"
    "v_exp_f32 v154, v154\n\t"
    "v_exp_f32 v155, v155\n\t"
    "v_sub_f32 v156, v156, v180\n\t"
    "v_sub_f32 v157, v157, v180\n\t"
    "v_exp_f32 v156, v156\n\t"
    "v_exp_f32 v157, v157\n\t"
    "v_add_f32 v182, v152, v154\n\t"
    "v_add_f32 v183, v153, v155\n\t"
    "v_sub_f32 v158, v158, v180\n\t"
    "v_sub_f32 v159, v159, v180\n\t"
    "v_exp_f32 v158, v158\n\t"
    "v_exp_f32 v159, v159\n\t"
    "v_add_f32 v182, v182, v156\n\t"
    "v_add_f32 v183, v183, v157\n\t"
    "v_sub_f32 v160, v160, v180\n\t"
    "v_sub_f32 v161, v161, v180\n\t"
    "v_exp_f32 v160, v160\n\t"
    "v_exp_f32 v161, v161\n\t"
    "v_add_f32 v182, v182, v158\n\t"
    "v_add_f32 v183, v183, v159\n\t"
    "v_sub_f32 v162, v162, v180\n\t"
    "v_sub_f32 v163, v163, v180\n\t"
    "v_exp_f32 v162, v162\n\t"
    "v_exp_f32 v163, v163\n\t"
    "v_add_f32 v182, v182, v160\n\t"
    "v_add_f32 v183, v183, v161\n\t"
    "v_sub_f32 v164, v164, v180\n\t"
    "v_sub_f32 v165, v165, v180\n\t"
    "v_exp_f32 v164, v164\n\t"
    "v_exp_f32 v165, v165\n\t"
    "v_add_f32 v182, v182, v162\n\t"
    "v_add_f32 v183, v183, v163\n\t"
    "v_sub_f32 v166, v166, v180\n\t"
    "v_sub_f32 v167, v167, v180\n\t"
    "v_exp_f32 v166, v166\n\t"
    "v_exp_f32 v167, v167\n\t"
    "v_add_f32 v182, v182, v164\n\t"
    "v_add_f32 v183, v183, v165\n\t"
    "ds_read_b32 v31, %[chk2]\n\t"
    "v_add_f32 v182, v182, v166\n\t"
    "v_add_f32 v183, v183, v167\n\t"
    "v_add_f32 v182, v182, v183\n\t"
    "v_mov_b32 v181, v182\n\t"
    "s_nop 1\n\t"
    "v_permlane16_swap_b32 v182, v181\n\t"
    "v_add_f32 v182, v182, v181\n\t"
    "v_mov_b32 v181, v182\n\t"
    "s_nop 1\n\t"
    "v_permlane32_swap_b32 v182, v181\n\t"
    "v_add_f32 v182, v182, v181\n\t"
    "v_rcp_f32 v182, v182\n\t"
    "s_waitcnt lgkmcnt(0)\n\t"
    "v_cmp_gt_u32 vcc, s61, v31\n\t"
    "s_cbranch_vccnz .Lsa7%=\n\t"
    ".Lra7%=:\n\t"
    "ds_read_b128 v[200:203], v29 offset:0\n\t"
    "v_mul_f32 v152, v152, v182\n\t"
    "ds_read_b128 v[204:207], v29 offset:2048\n\t"
    "v_mul_f32 v153, v153, v182\n\t"
    "ds_read_b128 v[208:211], v29 offset:4096\n\t"
    "v_mul_f32 v154, v154, v182\n\t"
    "ds_read_b128 v[212:215], v29 offset:6144\n\t"
    "v_mul_f32 v155, v155, v182\n\t"
    "ds_read_b128 v[216:219], v29 offset:8192\n\t"
    "v_mul_f32 v156, v156, v182\n\t"
    "ds_read_b128 v[220:223], v29 offset:10240\n\t"
    "v_mul_f32 v157, v157, v182\n\t"
    "ds_read_b128 v[224:227], v29 offset:12288\n\t"
    "v_mul_f32 v158, v158, v182\n\t"
    "ds_read_b128 v[228:231], v29 offset:14336\n\t"
    "v_mul_f32 v159, v159, v182\n\t"
    "ds_read_b128 v[232:235], v29 offset:16384\n\t"
    "v_cvt_pk_bf16_f32 v168, v152, v153\n\t"
    "ds_read_b128 v[236:239], v29 offset:18432\n\t"
    "v_cvt_pk_bf16_f32 v169, v154, v155\n\t"
    "ds_read_b128 v[240:243], v29 offset:20480\n\t"
    "v_cvt_pk_bf16_f32 v170, v156, v157\n\t"
    "ds_read_b128 v[244:247], v29 offset:22528\n\t"
    "v_cvt_pk_bf16_f32 v171, v158, v159\n\t"
    "s_setprio 2\n\t"
    "s_waitcnt lgkmcnt(8)\n\t"
    "v_mfma_f32_16x16x32_bf16 v[104:107], v[168:171], v[200:203], 0\n\t"
    "v_mul_f32 v160, v160, v182\n\t"
    "ds_read_b128 v[200:203], v29 offset:1024\n\t"
    "v_mfma_f32_16x16x32_bf16 v[108:111], v[168:171], v[204:207], 0\n\t"
    "v_mul_f32 v161, v161, v182\n\t"
    "ds_read_b128 v[204:207], v29 offset:3072\n\t"
    "v_mfma_f32_16x16x32_bf16 v[112:115], v[168:171], v[208:211], 0\n\t"
    "v_mul_f32 v162, v162, v182\n\t"
    "ds_read_b128 v[208:211], v29 offset:5120\n\t"
    "v_mfma_f32_16x16x32_bf16 v[116:119], v[168:171], v[212:215], 0\n\t"
    "v_mul_f32 v163, v163, v182\n\t"
    "ds_read_b128 v[212:215], v29 offset:7168\n\t"
    "s_waitcnt lgkmcnt(8)\n\t"
    "v_mfma_f32_16x16x32_bf16 v[120:123], v[168:171], v[216:219], 0\n\t"
    "v_mul_f32 v164, v164, v182\n\t"
    "ds_read_b128 v[216:219], v29 offset:9216\n\t"
    "v_mfma_f32_16x16x32_bf16 v[124:127], v[168:171], v[220:223], 0\n\t"
    "v_mul_f32 v165, v165, v182\n\t"
    "ds_read_b128 v[220:223], v29 offset:11264\n\t"
    "v_mfma_f32_16x16x32_bf16 v[128:131], v[168:171], v[224:227], 0\n\t"
    "v_mul_f32 v166, v166, v182\n\t"
    "ds_read_b128 v[224:227], v29 offset:13312\n\t"
    "v_mfma_f32_16x16x32_bf16 v[132:135], v[168:171], v[228:231], 0\n\t"
    "v_mul_f32 v167, v167, v182\n\t"
    "ds_read_b128 v[228:231], v29 offset:15360\n\t"
    "s_waitcnt lgkmcnt(8)\n\t"
    "v_mfma_f32_16x16x32_bf16 v[136:139], v[168:171], v[232:235], 0\n\t"
    "v_cvt_pk_bf16_f32 v172, v160, v161\n\t"
    "v_cvt_pk_bf16_f32 v173, v162, v163\n\t"
    "ds_read_b128 v[232:235], v29 offset:17408\n\t"
    "v_mfma_f32_16x16x32_bf16 v[140:143], v[168:171], v[236:239], 0\n\t"
    "v_cvt_pk_bf16_f32 v174, v164, v165\n\t"
    "v_cvt_pk_bf16_f32 v175, v166, v167\n\t"
    "ds_read_b128 v[236:239], v29 offset:19456\n\t"
    "v_mfma_f32_16x16x32_bf16 v[144:147], v[168:171], v[240:243], 0\n\t"
    "ds_read_b128 v[240:243], v29 offset:21504\n\t"
    "v_mfma_f32_16x16x32_bf16 v[148:151], v[168:171], v[244:247], 0\n\t"
    "ds_read_b128 v[244:247], v29 offset:23552\n\t"
    "s_waitcnt lgkmcnt(8)\n\t"
    "v_mfma_f32_16x16x32_bf16 v[104:107], v[172:175], v[200:203], v[104:107]\n\t"
    "ds_read_b128 v[200:203], %[xr0] offset:0\n\t"
    "v_mfma_f32_16x16x32_bf16 v[108:111], v[172:175], v[204:207], v[108:111]\n\t"
    "ds_read_b128 v[204:207], %[xr0] offset:2048\n\t"
    "v_mfma_f32_16x16x32_bf16 v[112:115], v[172:175], v[208:211], v[112:115]\n\t"
    "ds_read_b128 v[208:211], %[xr0] offset:4096\n\t"
    "v_mfma_f32_16x16x32_bf16 v[116:119], v[172:175], v[212:215], v[116:119]\n\t"
    "ds_read_b128 v[212:215], %[xr0] offset:6144\n\t"
    "s_waitcnt lgkmcnt(8)\n\t"
    "v_mfma_f32_16x16x32_bf16 v[120:123], v[172:175], v[216:219], v[120:123]\n\t"
    "ds_read_b128 v[216:219], %[xr1] offset:0\n\t"
    "v_mfma_f32_16x16x32_bf16 v[124:127], v[172:175], v[220:223], v[124:127]\n\t"
    "ds_read_b128 v[220:223], %[xr1] offset:2048\n\t"
    "v_mfma_f32_16x16x32_bf16 v[128:131], v[172:175], v[224:227], v[128:131]\n\t"
    "ds_read_b128 v[224:227], %[xr1] offset:4096\n\t"
    "v_mfma_f32_16x16x32_bf16 v[132:135], v[172:175], v[228:231], v[132:135]\n\t"
    "ds_read_b128 v[228:231], %[xr1] offset:6144\n\t"
    "s_waitcnt lgkmcnt(8)\n\t"
    "ds_write_b32 %[own], v28 offset:32\n\t"
    "v_mfma_f32_16x16x32_bf16 v[136:139], v[172:175], v[232:235], v[136:139]\n\t"
    "ds_read_b128 v[232:235], %[xr0] offset:8192\n\t"
    "v_mfma_f32_16x16x32_bf16 v[140:143], v[172:175], v[236:239], v[140:143]\n\t"
    "ds_read_b128 v[236:239], %[xr0] offset:10240\n\t"
    "v_mfma_f32_16x16x32_bf16 v[144:147], v[172:175], v[240:243], v[144:147]\n\t"
    "ds_read_b128 v[240:243], %[xr0] offset:12288\n\t"
    "v_mfma_f32_16x16x32_bf16 v[148:151], v[172:175], v[244:247], v[148:151]\n\t"
    "ds_read_b128 v[244:247], %[xr0] offset:14336\n\t"
    "s_setprio 0\n\t"
    "v_cvt_pk_bf16_f32 v248, v104, v108\n\t"
    "v_cvt_pk_bf16_f32 v249, v112, v116\n\t"
    "v_cvt_pk_bf16_f32 v250, v120, v124\n\t"
    "v_cvt_pk_bf16_f32 v251, v128, v132\n\t"
    "v_cvt_pk_bf16_f32 v252, v136, v140\n\t"
    "s_nop 0\n\t"
    "v_cvt_pk_bf16_f32 v253, v144, v148\n\t"
    "buffer_store_dwordx4 v[248:251], %[so4], %[ro], %[b2] offen sc1\n\t"
    "buffer_store_dwordx2 v[252:253], %[so2], %[ro], %[b2] offen sc1\n\t"
    "s_nop 0\n\t"
    "v_cvt_pk_bf16_f32 v248, v105, v109\n\t"
    "v_cvt_pk_bf16_f32 v249, v113, v117\n\t"
    "v_cvt_pk_bf16_f32 v250, v121, v125\n\t"
    "v_cvt_pk_bf16_f32 v251, v129, v133\n\t"
    "v_cvt_pk_bf16_f32 v252, v137, v141\n\t"
    "v_cvt_pk_bf16_f32 v253, v145, v149\n\t"
    "buffer_store_dwordx4 v[248:251], %[so4], %[ro], %[b2] offen offset:384 sc1\n\t"
    "buffer_store_dwordx2 v[252:253], %[so2], %[ro], %[b2] offen offset:384 sc1\n\t"
    "s_nop 0\n\t"
    "v_cvt_pk_bf16_f32 v248, v106, v110\n\t"
    "v_cvt_pk_bf16_f32 v249, v114, v118\n\t"
    "v_cvt_pk_bf16_f32 v250, v122, v126\n\t"
    "v_cvt_pk_bf16_f32 v251, v130, v134\n\t"
    "v_cvt_pk_bf16_f32 v252, v138, v142\n\t"
    "v_cvt_pk_bf16_f32 v253, v146, v150\n\t"
    "buffer_store_dwordx4 v[248:251], %[so4], %[ro], %[b2] offen offset:768 sc1\n\t"
    "buffer_store_dwordx2 v[252:253], %[so2], %[ro], %[b2] offen offset:768 sc1\n\t"
    "ds_read_b32 v30, %[chk1]\n\t"
    "v_cvt_pk_bf16_f32 v248, v107, v111\n\t"
    "v_cvt_pk_bf16_f32 v249, v115, v119\n\t"
    "v_cvt_pk_bf16_f32 v250, v123, v127\n\t"
    "v_cvt_pk_bf16_f32 v251, v131, v135\n\t"
    "v_cvt_pk_bf16_f32 v252, v139, v143\n\t"
    "v_cvt_pk_bf16_f32 v253, v147, v151\n\t"
    "buffer_store_dwordx4 v[248:251], %[so4], %[ro], %[b2] offen offset:1152 sc1\n\t"
    "buffer_store_dwordx2 v[252:253], %[so2], %[ro], %[b2] offen offset:1152 sc1\n\t"
    "s_add_u32 %[sj], %[sj], 3\n\t"
    "s_cmp_ge_u32 %[sj], 9\n\t"
    "s_cbranch_scc1 .Ltaila%=\n\t"
    "s_add_u32 %[b0], %[b0], 18874368\n\t"
    "s_add_u32 %[b1], %[b1], 18874368\n\t"
    "s_add_u32 %[b2], %[b2], 18874368\n\t"
    "s_branch .Lloopa%=\n\t"
    ".Ltaila%=:\n\t"
    "s_branch .Lenda%=\n\t"
    ".Lsa1%=:\n\t"
    "s_sleep 1\n\t"
    "ds_read_b32 v31, %[chk2] offset:0\n\t"
    "s_waitcnt lgkmcnt(0)\n\t"
    "v_cmp_gt_u32 vcc, s61, v31\n\t"
    "s_cbranch_vccnz .Lsa1%=\n\t"
    "s_branch .Lra1%=\n\t"
    ".Lsa2%=:\n\t"
    "s_sleep 1\n\t"
    "ds_read_b32 v30, %[chk1] offset:0\n\t"
    "s_waitcnt lgkmcnt(0)\n\t"
    "v_cmp_gt_u32 vcc, s61, v30\n\t"
    "s_cbranch_vccnz .Lsa2%=\n\t"
    "s_branch .Lra2%=\n\t"
    ".Lsa3%=:\n\t"
    "s_sleep 1\n\t"
    "ds_read_b32 v31, %[chk2] offset:0\n\t"
    "s_waitcnt lgkmcnt(0)\n\t"
    "v_cmp_gt_u32 vcc, s61, v31\n\t"
    "s_cbranch_vccnz .Lsa3%=\n\t"
    "s_branch .Lra3%=\n\t"
    ".Lsa4%=:\n\t"
    "s_sleep 1\n\t"
    "ds_read_b32 v30, %[chk1] offset:0\n\t"
    "s_waitcnt lgkmcnt(0)\n\t"
    "v_cmp_gt_u32 vcc, s61, v30\n\t"
    "s_cbranch_vccnz .Lsa4%=\n\t"
    "s_branch .Lra4%=\n\t"
    ".Lsa5%=:\n\t"
    "s_sleep 1\n\t"
    "ds_read_b32 v31, %[chk2] offset:0\n\t"
    "s_waitcnt lgkmcnt(0)\n\t"
    "v_cmp_gt_u32 vcc, s61, v31\n\t"
    "s_cbranch_vccnz .Lsa5%=\n\t"
    "s_branch .Lra5%=\n\t"
    ".Lsa6%=:\n\t"
    "s_sleep 1\n\t"
    "ds_read_b32 v30, %[chk1] offset:0\n\t"
    "s_waitcnt lgkmcnt(0)\n\t"
    "v_cmp_gt_u32 vcc, s61, v30\n\t"
    "s_cbranch_vccnz .Lsa6%=\n\t"
    "s_branch .Lra6%=\n\t"
    ".Lsa7%=:\n\t"
    "s_sleep 1\n\t"
    "ds_read_b32 v31, %[chk2] offset:0\n\t"
    "s_waitcnt lgkmcnt(0)\n\t"
    "v_cmp_gt_u32 vcc, s61, v31\n\t"
    "s_cbranch_vccnz .Lsa7%=\n\t"
    "s_branch .Lra7%=\n\t"
    ".Lenda%=:\n\t"
    : [b0] "+s"(b0), [b1] "+s"(b1), [b2] "+s"(b2), [sj] "+s"(sj)
    : [rx] "s"(rx), [ro] "s"(ro), [ld0] "v"(ld0), [ld1] "v"(ld1),
      [xw0] "v"(xw[0] + lb), [xw1] "v"(xw[1] + lb), [xw2] "v"(xw[2] + lb), [xw3] "v"(xw[3] + lb), [xw4] "v"(xw[4] + lb), [xw5] "v"(xw[5] + lb),
      [xr0] "v"(xr0), [xr1] "v"(xr1), [xs0] "v"(xr0 + 2 * SLOT), [xs1] "v"(xr1 + 2 * SLOT), [ta] "v"(ta), [va] "v"(va),
      [so4] "v"(so4), [so2] "v"(so2), [chk1] "v"(chk1), [chk2] "v"(chk2), [own] "v"(lb + LDS_F + 4 * a)
    : "memory", "vcc", "scc", "s60", "s61", "s62", "v28", "v29", "v30", "v31", "v32", "v33", "v34", "v35", "v36", "v37", "v38", "v39", "v40", "v41", "v42", "v43", "v44", "v45", "v46", "v47", "v48", "v49", "v50", "v51", "v52", "v53", "v54", "v55", "v56", "v57", "v58", "v59", "v60", "v61", "v62", "v63", "v64", "v65", "v66", "v67", "v68", "v69", "v70", "v71", "v72", "v73", "v74", "v75", "v76", "v77", "v78", "v79", "v80", "v81", "v82", "v83", "v84", "v85", "v86", "v87", "v88", "v89", "v90", "v91", "v92", "v93", "v94", "v95", "v96", "v97", "v98", "v99", "v100", "v101", "v102", "v103", "v104", "v105", "v106", "v107", "v108", "v109", "v110", "v111", "v112", "v113", "v114", "v115", "v116", "v117", "v118", "v119", "v120", "v121", "v122", "v123", "v124", "v125", "v126", "v127", "v128", "v129", "v130", "v131", "v132", "v133", "v134", "v135", "v136", "v137", "v138", "v139", "v140", "v141", "v142", "v143", "v144", "v145", "v146", "v147", "v148", "v149", "v150", "v151", "v152", "v153", "v154", "v155", "v156", "v157", "v158", "v159", "v160", "v161", "v162", "v163", "v164", "v165", "v166", "v167", "v168", "v169", "v170", "v171", "v172", "v173", "v174", "v175", "v176", "v177", "v178", "v179", "v180", "v181", "v182", "v183", "v184", "v185", "v186", "v187", "v188", "v189", "v190", "v191", "v192", "v193", "v194", "v195", "v196", "v197", "v198", "v199", "v200", "v201", "v202", "v203", "v204", "v205", "v206", "v207", "v208", "v209", "v210", "v211", "v212", "v213", "v214", "v215", "v216", "v217", "v218", "v219", "v220", "v221", "v222", "v223", "v224", "v225", "v226", "v227", "v228", "v229", "v230", "v231", "v232", "v233", "v234", "v235", "v236", "v237", "v238", "v239", "v240", "v241", "v242", "v243", "v244", "v245", "v246", "v247", "v248", "v249", "v250", "v251", "v252", "v253", "v254", "v255");
}

__global__ __launch_bounds__(512, 2) void swin_main(const char* __restrict__ x, const u32x4* __restrict__ frags, char* __restrict__ out) {
  extern __shared__ __attribute__((aligned(16))) char lds[];
  const int tid = threadIdx.x, lane = tid & 63, wid = __builtin_amdgcn_readfirstlane(tid >> 6);
  if (wid >= 4) attn_wave(lds, x, out, wid - 4, lane);
  else {
    if (wid == 0) *(lds_vu32*)(lds + LDS_F + lane * 4) = (lane >= 4 && lane < 12) ? 2u : (lane >= 12 && lane < 16) ? 1u : 0u;
    if (wid >= 2) proj_wave<1>(lds, frags, wid - 2, lane);
    else proj_wave<0>(lds, frags, wid, lane);
  }
}

extern "C" void kernel_launch(void* const* d_in, const int* in_sizes, int n_in,
                              void* d_out, int out_size, void* d_ws, size_t ws_size,
                              hipStream_t stream) {
  const char* x = (const char*)d_in[0];
  const unsigned short* wqkv = (const unsigned short*)d_in[1];
  const unsigned short* wo = (const unsigned short*)d_in[2];
  u32x4* frags = (u32x4*)d_ws;
  (void)hipFuncSetAttribute((const void*)swin_main, hipFuncAttributeMaxDynamicSharedMemorySize, LDS_TOTAL);
  prep_kernel<<<144, 64, 0, stream>>>(wqkv, wo, frags);
  swin_main<<<256, 512, LDS_TOTAL, stream>>>(x, frags, (char*)d_out);
}
```
